```python
import torch

def _softmax(x, axis=-1):
    return torch.softmax(x, dim=axis)
def _gelu(x):
    return torch.nn.functional.gelu(x, approximate="tanh")
def _silu(x):
    return torch.nn.functional.silu(x)
def _sigmoid(x):
    return torch.sigmoid(x)
def _layernorm(x, axis=-1, eps=1e-5):
    m = x.mean(dim=axis, keepdim=True)
    v = x.var(dim=axis, keepdim=True, unbiased=False)
    return (x - m) / torch.sqrt(v + eps)
def _scatter_add(tgt, idx, src, axis=0):
    return tgt.clone().index_add_(axis, idx, src)
def _segment_sum(src, seg, n):
    out = torch.zeros((n,) + src.shape[1:], dtype=src.dtype)
    return out.index_add_(0, seg, src)
def _pad(x, pw):
    flat = tuple(v for pair in reversed(pw) for v in pair)
    return torch.nn.functional.pad(x, flat)

kernel_name = 'standalone__low_rank_proj_linformer__prompt'

def setup_inputs():

    _w = (lambda a: (lambda t: t.to(torch.bfloat16) if t.is_floating_point() else t)(torch.as_tensor(a)))
    B, H, Sq, Skv, Dh, R = 1, 32, 2048, 2048, 192, 64
    return {
        "q": _w((lambda *s: torch.randn(*s, dtype=torch.bfloat16) * 0.8)(B, H, Sq, Dh)),
        "k": _w((lambda *s: torch.randn(*s, dtype=torch.bfloat16) * 0.8)(B, H, Skv, Dh)),
        "v": _w((lambda *s: torch.randn(*s, dtype=torch.bfloat16) * 0.05)(B, H, Skv, Dh)),
        "e_proj": _w((lambda *s: torch.randn(*s, dtype=torch.bfloat16) * 0.8)(R, Skv) * (Skv ** -0.5)),
        "f_proj": _w((lambda *s: torch.randn(*s, dtype=torch.bfloat16) * 0.05)(R, Skv) * (Skv ** -0.5)),
    }

def reference(q, k, v, e_proj, f_proj):
    B, H, Sq, Dh = q.shape
    kp = torch.einsum("rn,bhnd->bhrd", e_proj, k)
    vp = torch.einsum("rn,bhnd->bhrd", f_proj, v)
    scores = torch.einsum("bhqd,bhrd->bhqr", q, kp) * (Dh ** -0.5)
    p = _softmax(scores, axis=-1)
    o = torch.einsum("bhqr,bhrd->bhqd", p, vp)
    return o
```

```cpp
#include <hip/hip_runtime.h>
#include <hip/hip_bf16.h>

typedef short s16x4 __attribute__((ext_vector_type(4)));
typedef __bf16 bf16x8 __attribute__((ext_vector_type(8)));
typedef __bf16 bf16x2 __attribute__((ext_vector_type(2)));
typedef float f32x2 __attribute__((ext_vector_type(2)));
typedef float f32x4 __attribute__((ext_vector_type(4)));
typedef float f32x16 __attribute__((ext_vector_type(16)));
typedef unsigned u32x4 __attribute__((ext_vector_type(4)));
typedef unsigned u32x2 __attribute__((ext_vector_type(2)));
#define LAS __attribute__((address_space(3)))
#define GAS __attribute__((address_space(1)))
#define DI __device__ __forceinline__

constexpr int H = 32, SQ = 2048, SKV = 2048, DH = 192, R = 64;
constexpr int ROWB = DH * 2;
constexpr int HEAD_FRAG_BYTES = 48 * 1024;

DI unsigned pk2(float a, float b) {
  f32x2 f = {a, b};
  return __builtin_bit_cast(unsigned, __builtin_convertvector(f, bf16x2));
}
DI f32x4 mfma16(u32x4 a, u32x4 b, f32x4 c) {
  return __builtin_amdgcn_mfma_f32_16x16x32_bf16(__builtin_bit_cast(bf16x8, a), __builtin_bit_cast(bf16x8, b), c, 0, 0, 0);
}
DI f32x16 mfma32(u32x4 a, u32x4 b, f32x16 c) {
  return __builtin_amdgcn_mfma_f32_32x32x16_bf16(__builtin_bit_cast(bf16x8, a), __builtin_bit_cast(bf16x8, b), c, 0, 0, 0);
}

constexpr int P1_NW = 8;
constexpr int P1_STEPS = SKV / P1_NW / 32;
constexpr int P1_D = 4;
constexpr int P1_IMG = 3584;

template <bool ISV>
DI void proj_body(const char* __restrict__ src, const char* __restrict__ pm, char* __restrict__ outF, LAS char* sm, int c) {
  const int tid = threadIdx.x, lane = tid & 63, w = __builtin_amdgcn_readfirstlane(tid >> 6);
  unsigned koff[3], loff[3];
#pragma unroll
  for (int s = 0; s < 3; ++s) {
    const int i = lane + 64 * s, row = i / 6, ch = i - row * 6;
    koff[s] = (unsigned)(row * ROWB + ch * 16);
    loff[s] = (unsigned)(i * 16 + (row >> 3) * 128);
  }
  const GAS char* kb = (const GAS char*)src + (size_t)(w * (SKV / P1_NW)) * ROWB + c * 96;
  const GAS char* eb = (const GAS char*)pm + (size_t)(lane & 15) * (SKV * 2) + (w * (SKV / P1_NW) + 8 * (lane >> 4)) * 2;
  LAS char* img = sm + w * P1_IMG;
  const int g = lane >> 4, q = (lane & 15) >> 2, p = lane & 3;
  LAS char* trb = img + g * 896 + q * 96 + p * 8;

  u32x4 kr[P1_D][3], er[P1_D][4];
#pragma unroll
  for (int s = 0; s < P1_D; ++s) {
#pragma unroll
    for (int i = 0; i < 3; ++i) kr[s][i] = *(const GAS u32x4*)(kb + s * 32 * ROWB + koff[i]);
#pragma unroll
    for (int i = 0; i < 4; ++i) er[s][i] = *(const GAS u32x4*)(eb + i * 16 * (SKV * 2) + s * 64);
  }
  __builtin_amdgcn_sched_barrier(0);
  f32x4 acc[4][3];
#pragma unroll
  for (int i = 0; i < 4; ++i)
#pragma unroll
    for (int j = 0; j < 3; ++j) acc[i][j] = f32x4{0.f, 0.f, 0.f, 0.f};
#pragma unroll
  for (int s = 0; s < P1_STEPS; ++s) {
    const int b = s % P1_D;
#pragma unroll
    for (int i = 0; i < 3; ++i) *(LAS u32x4*)(img + loff[i]) = kr[b][i];
    u32x4 e[4];
#pragma unroll
    for (int i = 0; i < 4; ++i) e[i] = er[b][i];
    __builtin_amdgcn_wave_barrier();
    u32x4 kf[3];
#pragma unroll
    for (int j = 0; j < 3; ++j) {
      s16x4 lo = __builtin_amdgcn_ds_read_tr16_b64_v4i16((LAS s16x4*)(trb + j * 32));
      s16x4 hi = __builtin_amdgcn_ds_read_tr16_b64_v4i16((LAS s16x4*)(trb + j * 32 + 4 * 96));
      u32x2 l2 = __builtin_bit_cast(u32x2, lo), h2 = __builtin_bit_cast(u32x2, hi);
      kf[j] = u32x4{l2[0], l2[1], h2[0], h2[1]};
    }
    __builtin_amdgcn_wave_barrier();
#pragma unroll
    for (int i = 0; i < 4; ++i)
#pragma unroll
      for (int j = 0; j < 3; ++j) acc[i][j] = ISV ? mfma16(e[i], kf[j], acc[i][j]) : mfma16(kf[j], e[i], acc[i][j]);
    if (s + P1_D < P1_STEPS) {
#pragma unroll
      for (int i = 0; i < 3; ++i) kr[b][i] = *(const GAS u32x4*)(kb + (s + P1_D) * 32 * ROWB + koff[i]);
#pragma unroll
      for (int i = 0; i < 4; ++i) er[b][i] = *(const GAS u32x4*)(eb + i * 16 * (SKV * 2) + (s + P1_D) * 64);
    }
    __builtin_amdgcn_sched_barrier(0);
  }
  __syncthreads();
  LAS f32x4* red = (LAS f32x4*)sm;
#pragma unroll
  for (int i = 0; i < 4; ++i)
#pragma unroll
    for (int j = 0; j < 3; ++j) red[(w * 12 + i * 3 + j) * 64 + lane] = acc[i][j];
  __syncthreads();
  if (tid < 384) {
    int ia, ib, dst;
    if (!ISV) {
      const int f = tid >> 6, rt = f / 3, ct = f - rt * 3, r32 = lane & 31, hh = lane >> 5;
      const int r = rt * 32 + r32;
      ia = ((r >> 4) * 3 + ct) * 64 + (2 * hh) * 16 + (r & 15);
      ib = ia + 16;
      dst = ((rt * 12 + 3 * c + ct) * 64 + lane) * 16;
    } else {
      const int i16 = tid & 15, hh = (tid >> 4) & 1, ks = (tid >> 5) & 3, ct = tid >> 7;
      const int d16 = 8 * ((i16 >> 2) & 1) + (i16 & 3) + 4 * ((i16 >> 3) & 1);
      ia = (ks * 3 + ct) * 64 + hh * 16 + d16;
      ib = ia + 32;
      const int t16 = 3 * c + ct, dt = t16 >> 1, ihalf = t16 & 1;
      dst = 24 * 1024 + ((dt * 4 + ks) * 64 + hh * 32 + ihalf * 16 + i16) * 16;
    }
    f32x4 a = red[ia], b = red[ib];
#pragma unroll
    for (int ww = 1; ww < P1_NW; ++ww) {
      a += red[ww * 768 + ia];
      b += red[ww * 768 + ib];
    }
    u32x4 o = {pk2(a[0], a[1]), pk2(a[2], a[3]), pk2(b[0], b[1]), pk2(b[2], b[3])};
    *(GAS u32x4*)((GAS char*)outF + dst) = o;
  }
}

__global__ void __launch_bounds__(512, 2) proj_kernel(const char* __restrict__ k, const char* __restrict__ v, const char* __restrict__ e,
                                                      const char* __restrict__ f, char* __restrict__ ws) {
  __shared__ __attribute__((aligned(16))) char smem[P1_NW * 12 * 1024];
  const int b = blockIdx.x, xcd = b & 7, j = b >> 3, head = xcd * 4 + (j >> 3), sub = j & 7;
  const int c = sub & 3;
  const size_t hoff = (size_t)head * SKV * ROWB;
  char* outF = ws + (size_t)head * HEAD_FRAG_BYTES;
  if (sub >> 2) proj_body<true>(v + hoff, f, outF, (LAS char*)smem, c);
  else proj_body<false>(k + hoff, e, outF, (LAS char*)smem, c);
}

__global__ void __launch_bounds__(512, 2) attn_kernel(const char* __restrict__ q, const char* __restrict__ ws, char* __restrict__ out) {
  __shared__ __attribute__((aligned(16))) char smem[HEAD_FRAG_BYTES];
  const int b = blockIdx.x, xcd = b & 7, j = b >> 3, head = xcd * 4 + (j >> 3), sub = j & 7;
  const int tid = threadIdx.x, lane = tid & 63, w = __builtin_amdgcn_readfirstlane(tid >> 6);
  const int r32 = lane & 31, hh = lane >> 5;
  const size_t rowoff = ((size_t)head * SQ + sub * 256 + w * 32 + r32) * ROWB;
  const GAS char* qp = (const GAS char*)q + rowoff + hh * 16;
  const GAS u32x4* fr = (const GAS u32x4*)(ws + (size_t)head * HEAD_FRAG_BYTES);
  u32x4 st[6];
#pragma unroll
  for (int i = 0; i < 6; ++i) st[i] = fr[i * 512 + tid];
  u32x4 qf[12];
#pragma unroll
  for (int i = 0; i < 12; ++i) qf[i] = *(const GAS u32x4*)(qp + i * 32);
  __builtin_amdgcn_sched_barrier(0);
  LAS u32x4* sf = (LAS u32x4*)smem;
#pragma unroll
  for (int i = 0; i < 6; ++i) sf[i * 512 + tid] = st[i];
  __syncthreads();
  f32x16 s0, s1;
#pragma unroll
  for (int i = 0; i < 16; ++i) { s0[i] = 0.f; s1[i] = 0.f; }
#pragma unroll
  for (int ks = 0; ks < 12; ++ks) {
    s0 = mfma32(sf[ks * 64 + lane], qf[ks], s0);
    s1 = mfma32(sf[(12 + ks) * 64 + lane], qf[ks], s1);
  }
  float m = s0[0];
#pragma unroll
  for (int i = 1; i < 16; ++i) m = fmaxf(m, s0[i]);
#pragma unroll
  for (int i = 0; i < 16; ++i) m = fmaxf(m, s1[i]);
  m = fmaxf(m, __shfl_xor(m, 32));
  const float cs = 0.07216878364870322f * 1.4426950408889634f;
  const float mc = m * cs;
  float sum = 0.f;
#pragma unroll
  for (int i = 0; i < 16; ++i) { s0[i] = __builtin_amdgcn_exp2f(__builtin_fmaf(s0[i], cs, -mc)); sum += s0[i]; }
#pragma unroll
  for (int i = 0; i < 16; ++i) { s1[i] = __builtin_amdgcn_exp2f(__builtin_fmaf(s1[i], cs, -mc)); sum += s1[i]; }
  sum += __shfl_xor(sum, 32);
  const float inv = 1.0f / sum;
  u32x4 pf[4];
#pragma unroll
  for (int s = 0; s < 2; ++s) {
    pf[s] = u32x4{pk2(s0[8 * s] * inv, s0[8 * s + 1] * inv), pk2(s0[8 * s + 2] * inv, s0[8 * s + 3] * inv),
                  pk2(s0[8 * s + 4] * inv, s0[8 * s + 5] * inv), pk2(s0[8 * s + 6] * inv, s0[8 * s + 7] * inv)};
    pf[2 + s] = u32x4{pk2(s1[8 * s] * inv, s1[8 * s + 1] * inv), pk2(s1[8 * s + 2] * inv, s1[8 * s + 3] * inv),
                      pk2(s1[8 * s + 4] * inv, s1[8 * s + 5] * inv), pk2(s1[8 * s + 6] * inv, s1[8 * s + 7] * inv)};
  }
  GAS char* op = (GAS char*)out + rowoff + hh * 16;
#pragma unroll
  for (int dt = 0; dt < 6; ++dt) {
    f32x16 o;
#pragma unroll
    for (int i = 0; i < 16; ++i) o[i] = 0.f;
#pragma unroll
    for (int ks = 0; ks < 4; ++ks) o = mfma32(sf[(24 + dt * 4 + ks) * 64 + lane], pf[ks], o);
#pragma unroll
    for (int s = 0; s < 2; ++s) {
      u32x4 v = {pk2(o[8 * s], o[8 * s + 1]), pk2(o[8 * s + 2], o[8 * s + 3]), pk2(o[8 * s + 4], o[8 * s + 5]), pk2(o[8 * s + 6], o[8 * s + 7])};
      *(GAS u32x4*)(op + dt * 64 + s * 32) = v;
    }
  }
}

extern "C" void kernel_launch(void* const* d_in, const int* in_sizes, int n_in, void* d_out, int out_size, void* d_ws, size_t ws_size,
                              hipStream_t stream) {
  const char* q = (const char*)d_in[0];
  const char* k = (const char*)d_in[1];
  const char* v = (const char*)d_in[2];
  const char* e = (const char*)d_in[3];
  const char* f = (const char*)d_in[4];
  proj_kernel<<<256, 512, 0, stream>>>(k, v, e, f, (char*)d_ws);
  attn_kernel<<<256, 512, 0, stream>>>(q, (const char*)d_ws, (char*)d_out);
}
```

```cpp
#include <hip/hip_runtime.h>
#include <hip/hip_bf16.h>

typedef short s16x4 __attribute__((ext_vector_type(4)));
typedef __bf16 bf16x8 __attribute__((ext_vector_type(8)));
typedef __bf16 bf16x2 __attribute__((ext_vector_type(2)));
typedef float f32x2 __attribute__((ext_vector_type(2)));
typedef float f32x4 __attribute__((ext_vector_type(4)));
typedef float f32x16 __attribute__((ext_vector_type(16)));
typedef unsigned u32x4 __attribute__((ext_vector_type(4)));
typedef unsigned u32x2 __attribute__((ext_vector_type(2)));
typedef unsigned u32x3 __attribute__((ext_vector_type(3)));
#define LAS __attribute__((address_space(3)))
#define GAS __attribute__((address_space(1)))
#define DI __device__ __forceinline__
#define RLX_AGENT __ATOMIC_RELAXED, __HIP_MEMORY_SCOPE_AGENT
#define RLX_WG __ATOMIC_RELAXED, __HIP_MEMORY_SCOPE_WORKGROUP
typedef GAS unsigned gu32;

#define RING 4
#define QWIN 42
#define QPRE 2
#define DELTA 10
#define RETRY_SLEEP 32
#define DRAIN 1
#define STAMPS 0

constexpr int H = 32, SQ = 2048, SKV = 2048, DH = 192, R = 64;
constexpr int ROWB = DH * 2;
constexpr int SLOT = 16384;
constexpr int NST = 24;
constexpr int PART_OFF = 49152;
constexpr int PAY_HEAD = 2 * 49152;
constexpr int CTL_OFF = H * PAY_HEAD;
constexpr int CTL_HEAD = 72 * 128;
constexpr int DBG_OFF = CTL_OFF + H * CTL_HEAD + 4096;
constexpr unsigned POISON = 0xFFFFFFFFu;
constexpr unsigned SPIN_CAP = 1u << 20;
constexpr int VT = 4096 + 16;
constexpr int L_KP = 0, L_VP = 24576, L_SCR_HI = 49152 + 256, L_SCR_LO = L_SCR_HI + 49152, L_FLAG = L_SCR_LO + 49152, L_BYTES = L_FLAG + 64;
constexpr int F_HS = 0, F_KP = 1, F_VP = 2, F_Q = 3;

DI unsigned pk2(float a, float b) {
  f32x2 f = {a, b};
  return __builtin_bit_cast(unsigned, __builtin_convertvector(f, bf16x2));
}
DI f32x4 mfma16(u32x4 a, u32x4 b, f32x4 c) {
  return __builtin_amdgcn_mfma_f32_16x16x32_bf16(__builtin_bit_cast(bf16x8, a), __builtin_bit_cast(bf16x8, b), c, 0, 0, 0);
}
DI f32x16 mfma32(u32x4 a, u32x4 b, f32x16 c) {
  return __builtin_amdgcn_mfma_f32_32x32x16_bf16(__builtin_bit_cast(bf16x8, a), __builtin_bit_cast(bf16x8, b), c, 0, 0, 0);
}
DI u32x4 tr2(LAS char* p0, LAS char* p1) {
  u32x2 l2 = __builtin_bit_cast(u32x2, __builtin_amdgcn_ds_read_tr16_b64_v4i16((LAS s16x4*)p0));
  u32x2 h2 = __builtin_bit_cast(u32x2, __builtin_amdgcn_ds_read_tr16_b64_v4i16((LAS s16x4*)p1));
  return u32x4{l2[0], l2[1], h2[0], h2[1]};
}
DI void dma_nt(unsigned lds, unsigned voff, __amdgpu_buffer_rsrc_t rs, unsigned soff) {
  unsigned keep;
  asm volatile("s_mov_b32 %0, m0\n\ts_mov_b32 m0, %1\n\ts_nop 0\n\tbuffer_load_dwordx4 %2, %3, %4 offen nt lds\n\ts_mov_b32 m0, %0"
               : "=&s"(keep) : "s"(lds), "v"(voff), "s"(rs), "s"(soff) : "memory");
}
DI void dma(unsigned lds, unsigned voff, __amdgpu_buffer_rsrc_t rs, unsigned soff) {
  unsigned keep;
  asm volatile("s_mov_b32 %0, m0\n\ts_mov_b32 m0, %1\n\ts_nop 0\n\tbuffer_load_dwordx4 %2, %3, %4 offen lds\n\ts_mov_b32 m0, %0"
               : "=&s"(keep) : "s"(lds), "v"(voff), "s"(rs), "s"(soff) : "memory");
}
template <int N> DI void vmcnt() { asm volatile("s_waitcnt vmcnt(%0)" ::"n"(N) : "memory"); }
DI void lgkm0() { asm volatile("s_waitcnt lgkmcnt(0)" ::: "memory"); }
DI void bar() {
  lgkm0();
  __builtin_amdgcn_s_barrier();
  asm volatile("" ::: "memory");
}
template <int N> DI void bars() {
#pragma unroll
  for (int i = 0; i < N; ++i) __builtin_amdgcn_s_barrier();
  asm volatile("" ::: "memory");
}
DI u32x4 ld_sc1(__amdgpu_buffer_rsrc_t rs, unsigned voff, unsigned soff) { return __builtin_amdgcn_raw_buffer_load_b128(rs, voff, soff, 16); }
DI void st_sc1(u32x4 v, __amdgpu_buffer_rsrc_t rs, unsigned voff) { __builtin_amdgcn_raw_buffer_store_b128(v, rs, voff, 0, 16); }
DI unsigned addbf2(unsigned x, unsigned y) {
  const float lo = __uint_as_float(x << 16) + __uint_as_float(y << 16);
  const float hi = __uint_as_float(x & 0xffff0000u) + __uint_as_float(y & 0xffff0000u);
  return pk2(lo, hi);
}
DI unsigned umin2(unsigned a, unsigned b) { return a < b ? a : b; }
DI unsigned umax2(unsigned a, unsigned b) { return a > b ? a : b; }
DI unsigned umax4(u32x4 v) { return umax2(umax2(v[0], v[1]), umax2(v[2], v[3])); }
DI bool has_poison(u32x4 v) { return __builtin_amdgcn_ballot_w64(umax4(v) == POISON) != 0ull; }
DI u32x4 clean(u32x4 v) { return u32x4{umin2(v[0], POISON - 1u), umin2(v[1], POISON - 1u), umin2(v[2], POISON - 1u), umin2(v[3], POISON - 1u)}; }
DI void lds_wait(LAS unsigned* p, unsigned want) {
#pragma unroll 1
  for (unsigned spins = 0; spins < (SPIN_CAP << 6); ++spins) {
    if ((unsigned)__builtin_amdgcn_readfirstlane(__hip_atomic_load(p, RLX_WG)) >= want) break;
    __builtin_amdgcn_s_sleep(1);
  }
  asm volatile("" ::: "memory");
}
#define STAMP(i) do { } while (0)

__global__ void __launch_bounds__(576, 3) lin_kernel(const char* __restrict__ q, const char* __restrict__ k, const char* __restrict__ v,
                                                     const char* __restrict__ e, const char* __restrict__ f, char* ws,
                                                     char* __restrict__ out) {
  __shared__ __attribute__((aligned(1024))) char smem[L_BYTES];
  LAS char* L = (LAS char*)smem;
  LAS unsigned* flag = (LAS unsigned*)(L + L_FLAG);
  STAMP(0);
  const int b = blockIdx.x, xcd = b & 7, jj = b >> 3, head = xcd * 4 + (jj >> 3), sub = jj & 7;
  const int tensor = sub >> 2, role = sub & 3;
  const int tid = threadIdx.x, lane = tid & 63, w = __builtin_amdgcn_readfirstlane(tid >> 6);
  const bool helper = role == 3;
  const int lw = w & 3;
  const unsigned ldsb = (unsigned)(size_t)L;
  const __amdgpu_buffer_rsrc_t rsP = __builtin_amdgcn_make_buffer_rsrc(ws + (size_t)head * PAY_HEAD, 0, PAY_HEAD, 0x00020000);
  gu32* ctl = (gu32*)(ws + CTL_OFF + head * CTL_HEAD);

  if (w == 8) {
    gu32* inbox_p = ctl + (8 + sub * 8 + (lane & 7)) * 32;
    gu32* nonce_p = ctl + (lane & 7) * 32;
    gu32* ack_p = ctl + (8 + (lane & 7) * 8 + sub) * 32;
    const unsigned old = __hip_atomic_load(lane < 8 ? inbox_p : ctl + sub * 32, RLX_AGENT);
    if (lane < 8) flag[lane] = 0u;
    const u32x4 pz = {POISON, POISON, POISON, POISON};
    lgkm0();
    bars<1>();
    unsigned n = __builtin_amdgcn_readlane(old, 16) + 1u;
    while (__builtin_amdgcn_ballot_w64(old == n) != 0ull) ++n;
    if (lane == 0) __hip_atomic_store(ctl + sub * 32, n, RLX_AGENT);
#pragma unroll 1
    for (int s = 0; s < 3; ++s) {
      if (helper || s == 0) {
        const unsigned base = lane * 16 + tensor * 24576 + (helper ? PART_OFF + s * 4096 : role * 4096);
#pragma unroll
        for (int i = 0; i < 8; ++i) st_sc1(pz, rsP, base + (i >> 2) * 12288 + (i & 3) * 1024);
      }
      bars<1>();
    }
    bars<2>();
    unsigned echoed = __hip_atomic_load(nonce_p, RLX_AGENT);
    bars<1>();
    vmcnt<1>();
    bars<3>();
    vmcnt<0>();
    if (lane < 8) __hip_atomic_store(ack_p, echoed, RLX_AGENT);
    bars<4>();
    unsigned inbox = __hip_atomic_load(inbox_p, RLX_AGENT);
    bars<4>();
    vmcnt<0>();
    const bool fast = __builtin_amdgcn_ballot_w64(inbox != n) == 0ull;
    unsigned cur = __hip_atomic_load(nonce_p, RLX_AGENT);
    bars<3>();
    if (lane < 8 && cur != echoed) __hip_atomic_store(ack_p, cur, RLX_AGENT);
    echoed = cur;
    if (fast && lane == 0) flag[F_HS] = 1u;
    lgkm0();
    const __amdgpu_buffer_rsrc_t rsQ =
        __builtin_amdgcn_make_buffer_rsrc((char*)q + ((size_t)head * SQ + sub * 256) * ROWB, 0, 256 * ROWB, 0x00020000);
    unsigned voQ[6];
#pragma unroll
    for (int bb = 0; bb < 3; ++bb) {
      const int idx = 64 * bb + lane, row = idx / 24, c = idx - row * 24;
      voQ[bb] = row * 384 + (((c & 24) | ((c & 7) ^ (row >> 1))) << 4);
      voQ[3 + bb] = (voQ[bb] ^ 64u) + 3072u;
    }
    auto qgroup = [&](int j) {
      const unsigned d = ldsb + (j < 8 ? L_SCR_LO : L_SCR_HI - 49152) + j * 6144;
#pragma unroll
      for (int i = 0; i < 6; ++i) dma_nt(d + i * 1024, voQ[i], rsQ, j * 6144);
    };
    bars<NST - 1 - 21>();
    int j = 0;
#pragma unroll 1
    for (; j < QPRE; ++j) qgroup(j);
    bars<1>();
#pragma unroll 1
    for (; j < 2 * QPRE; ++j) qgroup(j);
    if (!fast) {
#pragma unroll 1
      for (unsigned spins = 0; spins < SPIN_CAP; ++spins) {
        inbox = __hip_atomic_load(inbox_p, RLX_AGENT);
        vmcnt<0>();
        cur = __hip_atomic_load(nonce_p, RLX_AGENT);
        if (lane < 8 && cur != echoed) __hip_atomic_store(ack_p, cur, RLX_AGENT);
        echoed = cur;
        if (__builtin_amdgcn_ballot_w64(inbox != n) == 0ull) break;
        __builtin_amdgcn_s_sleep(2);
      }
      if (lane == 0) __hip_atomic_store(flag + F_HS, 1u, RLX_WG);
    }
    bars<1>();
#pragma unroll 1
    for (; j < 16; ++j) {
      qgroup(j);
      vmcnt<QWIN>();
      const int landed = (6 * (j + 1) - QWIN) / 6;
      if (landed > 0 && lane == 0) __hip_atomic_store(flag + F_Q, (unsigned)landed, RLX_WG);
    }
    if (QWIN > 36) {
      vmcnt<36>();
      if (lane == 0) __hip_atomic_store(flag + F_Q, 10u, RLX_WG);
    }
    if (QWIN > 24) {
      vmcnt<24>();
      if (lane == 0) __hip_atomic_store(flag + F_Q, 12u, RLX_WG);
    }
    if (QWIN > 12) {
      vmcnt<12>();
      if (lane == 0) __hip_atomic_store(flag + F_Q, 14u, RLX_WG);
    }
    vmcnt<0>();
    if (lane == 0) __hip_atomic_store(flag + F_Q, 16u, RLX_WG);
    return;
  }

  const __amdgpu_buffer_rsrc_t rsK =
      __builtin_amdgcn_make_buffer_rsrc((char*)(tensor ? v : k) + (size_t)head * SKV * ROWB, 0, SKV * ROWB, 0x00020000);
  const __amdgpu_buffer_rsrc_t rsE = __builtin_amdgcn_make_buffer_rsrc((char*)(tensor ? f : e), 0, R * SKV * 2, 0x00020000);
  unsigned sk = helper ? 1536u * ROWB : role * 128u, se = helper ? 3072u : 0u;
  unsigned voK[2], voE[2];
#pragma unroll
  for (int x = 0; x < 2; ++x) {
    const int row = 8 * (2 * lw + x) + (lane >> 3);
    voK[x] = row * ROWB + (((lane & 7) ^ ((((lane >> 4) & 1) | (x << 1)) << 1)) << 4);
    voE[x] = row * (SKV * 2) + (((lane & 7) ^ ((x << 2) | (lane >> 4))) << 4);
  }
  unsigned dsl = ldsb + lw * 2048;
  auto issue = [&]() {
    dma_nt(dsl, voK[0], rsK, sk);
    dma_nt(dsl + 1024, voK[1], rsK, sk);
    dma(dsl + 8192, voE[0], rsE, se);
    dma(dsl + 9216, voE[1], rsE, se);
    sk += 64u * ROWB;
    se += 128u;
    if (se == 4096u) { se = 3072u; sk -= 512u * ROWB - 128u; }
    dsl += SLOT;
    if (dsl >= ldsb + RING * SLOT) dsl -= RING * SLOT;
  };
  if (w >= 4) {
#pragma unroll
    for (int s = 0; s < RING - 1; ++s) issue();
  }
  const int g = lane >> 4, q4 = (lane & 15) >> 2, p4 = lane & 3, r16 = lane & 15;
  const int r32 = lane & 31, hh = lane >> 5;
  const int rt16 = w & 3, ch = w >> 2;
  const unsigned fin_v = tensor ? 24576 + ch * 12288 + rt16 * 1024 + lane * 16
                                : (((rt16 >> 1) * 12 + 2 * ch + (g & 1)) * 64 + (g >> 1) * 32 + (rt16 & 1) * 16 + r16) * 16;
  const int fk = (q4 >> 1) | ((g & 1) << 1);
  const unsigned kt0 = (8 * g + q4) * 128 + (((2 * ch) ^ fk) << 5) + p4 * 8;
  const unsigned kt1 = (8 * g + q4) * 128 + (((2 * ch + 1) ^ fk) << 5) + p4 * 8;
  const unsigned et0 = 8192 + (16 * rt16 + r16) * 128 + ((g ^ (r16 >> 1)) << 4);
  const unsigned et1 = 8192 + (16 * rt16 + r16) * 128 + (((4 + g) ^ (r16 >> 1)) << 4);
  f32x4 acc0 = {0.f, 0.f, 0.f, 0.f}, acc1 = {0.f, 0.f, 0.f, 0.f};
  STAMP(1);
  int so = 0;
  auto compute = [&]() {
    LAS char* sl = L + so;
    const u32x4 a00 = tr2(sl + kt0, sl + kt0 + 512), a01 = tr2(sl + kt1, sl + kt1 + 512);
    const u32x4 b0 = *(LAS u32x4*)(sl + et0);
    const u32x4 a10 = tr2(sl + kt0 + 4096, sl + kt0 + 4608), a11 = tr2(sl + kt1 + 4096, sl + kt1 + 4608);
    const u32x4 b1 = *(LAS u32x4*)(sl + et1);
    if (tensor) {
      acc0 = mfma16(b0, a00, acc0);
      acc1 = mfma16(b0, a01, acc1);
      acc0 = mfma16(b1, a10, acc0);
      acc1 = mfma16(b1, a11, acc1);
    } else {
      acc0 = mfma16(a00, b0, acc0);
      acc1 = mfma16(a01, b0, acc1);
      acc0 = mfma16(a10, b1, acc0);
      acc1 = mfma16(a11, b1, acc1);
    }
    so += SLOT;
    if (so == RING * SLOT) so = 0;
  };
  auto cross = [&](u32x4 x) {
    if (tensor) {
      auto r0 = __builtin_amdgcn_permlane32_swap(x[0], x[2], false, false);
      auto r1 = __builtin_amdgcn_permlane32_swap(x[1], x[3], false, false);
      return u32x4{r0[0], r1[0], r0[1], r1[1]};
    }
    auto r0 = __builtin_amdgcn_permlane16_swap(x[0], x[2], false, false);
    auto r1 = __builtin_amdgcn_permlane16_swap(x[1], x[3], false, false);
    return u32x4{r0[0], r1[0], r0[1], r1[1]};
  };
  auto publish = [&](unsigned off) {
    st_sc1(clean(cross(u32x4{pk2(acc0[0], acc0[1]), pk2(acc0[2], acc0[3]), pk2(acc1[0], acc1[1]), pk2(acc1[2], acc1[3])})), rsP, fin_v + off);
  };
#define STAGE_HEAD(REM, XTRA)                                                  \
  do {                                                                         \
    if (w >= 4) vmcnt<4 * ((REM) < RING - 2 ? (REM) : RING - 2) + (XTRA)>();   \
    bar();                                                                     \
    if ((REM) >= RING - 1 && w >= 4) issue();                                  \
  } while (0)
#define STAGE(REM, XTRA) do { STAGE_HEAD(REM, XTRA); compute(); } while (0)
  static_assert(RING >= 4 && RING <= 6, "the waits below are written for this range");
#pragma unroll 1
  for (int s = 0; s < NST - 6; ++s) {
    STAGE(6, 0);
    if (helper && (s & 7) == 7) {
      publish(PART_OFF + (s >> 3) * 4096);
      acc0 = f32x4{0.f, 0.f, 0.f, 0.f};
      acc1 = f32x4{0.f, 0.f, 0.f, 0.f};
    }
  }
  STAGE(5, 0);
  STAGE(4, 0);
  STAGE(3, 0);
  STAGE_HEAD(2, 0);
  const unsigned hs_seen = __hip_atomic_load(flag + F_HS, RLX_WG);
  const unsigned part_c = role < 2 ? PART_OFF + role * 4096 : PAY_HEAD;
  u32x4 pa = ld_sc1(rsP, fin_v, part_c);
  compute();
  STAGE(1, 1);
  STAGE(0, 1);
  STAMP(3);
  if (__builtin_amdgcn_readfirstlane(hs_seen) == 0u) {
    lds_wait(flag + F_HS, 1u);
    pa[0] = POISON;
  }
#pragma unroll 1
  for (unsigned spins = 0; spins < SPIN_CAP; ++spins) {
    if (!has_poison(pa)) break;
    __builtin_amdgcn_s_sleep(8);
    asm volatile("" ::: "memory");
    pa = ld_sc1(rsP, fin_v, part_c);
  }
  pa = cross(pa);
  acc0 += f32x4{__uint_as_float(pa[0] << 16), __uint_as_float(pa[0] & 0xffff0000u), __uint_as_float(pa[1] << 16), __uint_as_float(pa[1] & 0xffff0000u)};
  acc1 += f32x4{__uint_as_float(pa[2] << 16), __uint_as_float(pa[2] & 0xffff0000u), __uint_as_float(pa[3] << 16), __uint_as_float(pa[3] & 0xffff0000u)};
  publish(helper ? PART_OFF + 8192 : role * 4096);
  asm volatile("" ::: "memory");
  bar();
  STAMP(2);
  {
    const unsigned go = lw * 1024 + lane * 16, gs = ch * 24576;
    unsigned dst[6];
#pragma unroll
    for (int i = 0; i < 6; ++i) {
      const unsigned d = 64 * (i % 3) + 32 * (i / 3) + 16 * hh + r16, d6 = d / 6u;
      dst[i] = ch ? L_VP + (d - 6 * d6) * VT + lw * 1024 + (g & 1) * 512 + d6 * 16 : L_KP + i * 4096 + go;
    }
    if (DRAIN) vmcnt<0>();
    if (DELTA) __builtin_amdgcn_s_sleep(DELTA);
    STAMP(11);
    unsigned pend = 7u;
#pragma unroll 1
    for (unsigned spins = 0; spins < SPIN_CAP; ++spins) {
      asm volatile("" ::: "memory");
      u32x4 gv[8];
#pragma unroll
      for (int i = 0; i < 6; ++i) gv[i] = ld_sc1(rsP, go, (pend >> (i % 3)) & 1u ? gs + i * 4096 : PAY_HEAD);
      gv[6] = ld_sc1(rsP, go, pend & 4u ? gs + PART_OFF + 2 * 4096 : PAY_HEAD);
      gv[7] = ld_sc1(rsP, go, pend & 4u ? gs + PART_OFF + 5 * 4096 : PAY_HEAD);
      asm volatile("" : "+v"(gv[0]), "+v"(gv[1]), "+v"(gv[2]), "+v"(gv[3]), "+v"(gv[4]), "+v"(gv[5]), "+v"(gv[6]), "+v"(gv[7]));
#pragma unroll
      for (int c = 0; c < 2; ++c) {
        if (((pend >> c) & 1u) && __builtin_amdgcn_ballot_w64(umax2(umax4(gv[c]), umax4(gv[3 + c])) == POISON) == 0ull) {
          *(LAS u32x4*)(L + dst[c]) = gv[c];
          *(LAS u32x4*)(L + dst[3 + c]) = gv[3 + c];
          pend &= ~(1u << c);
        }
      }
      if ((pend & 4u) && __builtin_amdgcn_ballot_w64(umax2(umax2(umax4(gv[2]), umax4(gv[5])), umax2(umax4(gv[6]), umax4(gv[7]))) == POISON) == 0ull) {
        *(LAS u32x4*)(L + dst[2]) =
            u32x4{addbf2(gv[2][0], gv[6][0]), addbf2(gv[2][1], gv[6][1]), addbf2(gv[2][2], gv[6][2]), addbf2(gv[2][3], gv[6][3])};
        *(LAS u32x4*)(L + dst[5]) =
            u32x4{addbf2(gv[5][0], gv[7][0]), addbf2(gv[5][1], gv[7][1]), addbf2(gv[5][2], gv[7][2]), addbf2(gv[5][3], gv[7][3])};
        pend &= 3u;
      }
      if (pend == 0u) break;
      if (RETRY_SLEEP) __builtin_amdgcn_s_sleep(RETRY_SLEEP);
    }
    lgkm0();
    if (lane == 0) __hip_atomic_fetch_add(flag + F_KP + ch, 1u, RLX_WG);
  }
  STAMP(4);
  LAS char* scr = L + (w < 4 ? L_SCR_LO : L_SCR_HI) + lw * 12288;
  unsigned fa[4];
#pragma unroll
  for (int k4 = 0; k4 < 4; ++k4) fa[k4] = r32 * 384 + (((2 * k4 + hh) ^ ((r32 >> 1) & 7)) << 4);
  lds_wait(flag + F_Q, 2 * w + 2);
  STAMP(5);
  u32x4 qf[12];
#pragma unroll
  for (int ks = 0; ks < 12; ++ks) qf[ks] = *(LAS u32x4*)(scr + fa[ks & 3] + (ks >> 2) * 128);
  lds_wait(flag + F_KP, 4u);
  STAMP(6);

  f32x16 s0, s1;
#pragma unroll
  for (int i = 0; i < 16; ++i) { s0[i] = 0.f; s1[i] = 0.f; }
  {
    LAS u32x4* kf = (LAS u32x4*)(L + L_KP) + lane;
    u32x4 ka[24];
#pragma unroll
    for (int i = 0; i < 6; ++i) ka[i] = kf[i * 64];
    __builtin_amdgcn_sched_barrier(0);
#pragma unroll
    for (int i = 0; i < 24; ++i) {
      if (i + 6 < 24) ka[i + 6] = kf[(i + 6) * 64];
      if (i < 12) s0 = mfma32(ka[i], qf[i], s0);
      else s1 = mfma32(ka[i], qf[i - 12], s1);
      __builtin_amdgcn_sched_barrier(0);
    }
  }
  float m = s0[0];
#pragma unroll
  for (int i = 1; i < 16; ++i) m = fmaxf(m, s0[i]);
#pragma unroll
  for (int i = 0; i < 16; ++i) m = fmaxf(m, s1[i]);
  {
    auto rr = __builtin_amdgcn_permlane32_swap(__float_as_uint(m), __float_as_uint(m), false, false);
    m = fmaxf(__uint_as_float(rr[0]), __uint_as_float(rr[1]));
  }
  const float cs = 0.07216878364870322f * 1.4426950408889634f;
  const float mc = m * cs;
  float sum = 0.f;
#pragma unroll
  for (int i = 0; i < 16; ++i) { s0[i] = __builtin_amdgcn_exp2f(__builtin_fmaf(s0[i], cs, -mc)); sum += s0[i]; }
#pragma unroll
  for (int i = 0; i < 16; ++i) { s1[i] = __builtin_amdgcn_exp2f(__builtin_fmaf(s1[i], cs, -mc)); sum += s1[i]; }
  {
    auto rr = __builtin_amdgcn_permlane32_swap(__float_as_uint(sum), __float_as_uint(sum), false, false);
    sum = __uint_as_float(rr[0]) + __uint_as_float(rr[1]);
  }
  const float inv = 1.0f / sum;
  u32x4 pf[4];
#pragma unroll
  for (int t = 0; t < 2; ++t) {
    pf[t] = u32x4{pk2(s0[8 * t] * inv, s0[8 * t + 1] * inv), pk2(s0[8 * t + 2] * inv, s0[8 * t + 3] * inv),
                  pk2(s0[8 * t + 4] * inv, s0[8 * t + 5] * inv), pk2(s0[8 * t + 6] * inv, s0[8 * t + 7] * inv)};
    pf[2 + t] = u32x4{pk2(s1[8 * t] * inv, s1[8 * t + 1] * inv), pk2(s1[8 * t + 2] * inv, s1[8 * t + 3] * inv),
                      pk2(s1[8 * t + 4] * inv, s1[8 * t + 5] * inv), pk2(s1[8 * t + 6] * inv, s1[8 * t + 7] * inv)};
  }
  STAMP(7);
  lds_wait(flag + F_VP, 4u);
  STAMP(8);
  f32x16 o[6];
#pragma unroll
  for (int t = 0; t < 6; ++t)
#pragma unroll
    for (int i = 0; i < 16; ++i) o[t][i] = 0.f;
  {
    LAS char* vb = L + L_VP + lane * 16;
    u32x4 va[24];
#pragma unroll
    for (int i = 0; i < 6; ++i) va[i] = *(LAS u32x4*)(vb + (i % 6) * VT + (i / 6) * 1024);
    __builtin_amdgcn_sched_barrier(0);
#pragma unroll
    for (int i = 0; i < 24; ++i) {
      if (i + 6 < 24) va[i + 6] = *(LAS u32x4*)(vb + ((i + 6) % 6) * VT + ((i + 6) / 6) * 1024);
      o[i % 6] = mfma32(pf[i / 6], va[i], o[i % 6]);
      __builtin_amdgcn_sched_barrier(0);
    }
  }
  const __amdgpu_buffer_rsrc_t rsO =
      __builtin_amdgcn_make_buffer_rsrc(out + ((size_t)head * SQ + sub * 256 + w * 32) * ROWB, 0, 32 * ROWB, 0x00020000);
  const unsigned ov = hh * 4 * ROWB + r32 * 12;
#pragma unroll
  for (int j = 0; j < 16; ++j) {
    const u32x3 x = {pk2(o[0][j], o[1][j]), pk2(o[2][j], o[3][j]), pk2(o[4][j], o[5][j])};
    __builtin_amdgcn_raw_buffer_store_b96(x, rsO, ov + ((j & 3) + 8 * (j >> 2)) * ROWB, 0, 2);
  }
}

extern "C" void kernel_launch(void* const* d_in, const int* in_sizes, int n_in, void* d_out, int out_size, void* d_ws, size_t ws_size,
                              hipStream_t stream) {
  lin_kernel<<<256, 576, 0, stream>>>((const char*)d_in[0], (const char*)d_in[1], (const char*)d_in[2], (const char*)d_in[3],
                                      (const char*)d_in[4], (char*)d_ws, (char*)d_out);
}
```

```cpp
#include <hip/hip_runtime.h>
#include <hip/hip_bf16.h>

typedef short s16x4 __attribute__((ext_vector_type(4)));
typedef __bf16 bf16x8 __attribute__((ext_vector_type(8)));
typedef __bf16 bf16x2 __attribute__((ext_vector_type(2)));
typedef float f32x2 __attribute__((ext_vector_type(2)));
typedef float f32x4 __attribute__((ext_vector_type(4)));
typedef float f32x16 __attribute__((ext_vector_type(16)));
typedef unsigned u32x4 __attribute__((ext_vector_type(4)));
typedef unsigned u32x2 __attribute__((ext_vector_type(2)));
typedef unsigned u32x3 __attribute__((ext_vector_type(3)));
#define LAS __attribute__((address_space(3)))
#define GAS __attribute__((address_space(1)))
#define DI __device__ __forceinline__
#define RLX_AGENT __ATOMIC_RELAXED, __HIP_MEMORY_SCOPE_AGENT
#define RLX_WG __ATOMIC_RELAXED, __HIP_MEMORY_SCOPE_WORKGROUP
typedef GAS unsigned gu32;

#define RING 5
#define QHOLD 8
#define QPRE 2
#define POLLGAP 3
#define RETRY_SLEEP 32
#define ROT 0
#define STAMPS 0

constexpr int H = 32, SQ = 2048, SKV = 2048, DH = 192, R = 64;
constexpr int ROWB = DH * 2;
constexpr int SLOT = 16384;
constexpr int NST = 24;
constexpr int PART_OFF = 49152;
constexpr int PAY_HEAD = 2 * 49152;
constexpr int CTL_OFF = H * PAY_HEAD;
constexpr int CNT_W = 72 * 32;
constexpr int CTL_HEAD = (72 + 16) * 128;
constexpr int DBG_OFF = CTL_OFF + H * CTL_HEAD + 4096;
constexpr unsigned POISON = 0xFFFFFFFFu;
constexpr unsigned SPIN_CAP = 1u << 20;
constexpr int VT = 4096 + 16;
constexpr int L_KP = 0, L_VP = 24576, L_SCR_HI = 49152 + 256, L_SCR_LO = L_SCR_HI + 49152, L_FLAG = L_SCR_LO + 49152, L_BYTES = L_FLAG + 64;
constexpr int F_HS = 0, F_KP = 1, F_VP = 2, F_Q = 3, F_ISS = 4, F_GO = 5  ;

DI unsigned pk2(float a, float b) {
  f32x2 f = {a, b};
  return __builtin_bit_cast(unsigned, __builtin_convertvector(f, bf16x2));
}
DI f32x4 mfma16(u32x4 a, u32x4 b, f32x4 c) {
  return __builtin_amdgcn_mfma_f32_16x16x32_bf16(__builtin_bit_cast(bf16x8, a), __builtin_bit_cast(bf16x8, b), c, 0, 0, 0);
}
DI f32x16 mfma32(u32x4 a, u32x4 b, f32x16 c) {
  return __builtin_amdgcn_mfma_f32_32x32x16_bf16(__builtin_bit_cast(bf16x8, a), __builtin_bit_cast(bf16x8, b), c, 0, 0, 0);
}
DI u32x4 tr2(LAS char* p0, LAS char* p1) {
  u32x2 l2 = __builtin_bit_cast(u32x2, __builtin_amdgcn_ds_read_tr16_b64_v4i16((LAS s16x4*)p0));
  u32x2 h2 = __builtin_bit_cast(u32x2, __builtin_amdgcn_ds_read_tr16_b64_v4i16((LAS s16x4*)p1));
  return u32x4{l2[0], l2[1], h2[0], h2[1]};
}
DI void dma_nt(unsigned lds, unsigned voff, __amdgpu_buffer_rsrc_t rs, unsigned soff) {
  unsigned keep;
  asm volatile("s_mov_b32 %0, m0\n\ts_mov_b32 m0, %1\n\ts_nop 0\n\tbuffer_load_dwordx4 %2, %3, %4 offen nt lds\n\ts_mov_b32 m0, %0"
               : "=&s"(keep) : "s"(lds), "v"(voff), "s"(rs), "s"(soff) : "memory");
}
DI void dma(unsigned lds, unsigned voff, __amdgpu_buffer_rsrc_t rs, unsigned soff) {
  unsigned keep;
  asm volatile("s_mov_b32 %0, m0\n\ts_mov_b32 m0, %1\n\ts_nop 0\n\tbuffer_load_dwordx4 %2, %3, %4 offen lds\n\ts_mov_b32 m0, %0"
               : "=&s"(keep) : "s"(lds), "v"(voff), "s"(rs), "s"(soff) : "memory");
}
template <int N> DI void vmcnt() { asm volatile("s_waitcnt vmcnt(%0)" ::"n"(N) : "memory"); }
DI void lgkm0() { asm volatile("s_waitcnt lgkmcnt(0)" ::: "memory"); }
DI void bar() {
  lgkm0();
  __builtin_amdgcn_s_barrier();
  asm volatile("" ::: "memory");
}
template <int N> DI void bars() {
#pragma unroll
  for (int i = 0; i < N; ++i) __builtin_amdgcn_s_barrier();
  asm volatile("" ::: "memory");
}
DI u32x4 ld_sc1(__amdgpu_buffer_rsrc_t rs, unsigned voff, unsigned soff) { return __builtin_amdgcn_raw_buffer_load_b128(rs, voff, soff, 16); }
DI void st_sc1(u32x4 v, __amdgpu_buffer_rsrc_t rs, unsigned voff) { __builtin_amdgcn_raw_buffer_store_b128(v, rs, voff, 0, 16); }
DI unsigned addbf2(unsigned x, unsigned y) {
  const float lo = __uint_as_float(x << 16) + __uint_as_float(y << 16);
  const float hi = __uint_as_float(x & 0xffff0000u) + __uint_as_float(y & 0xffff0000u);
  return pk2(lo, hi);
}
DI unsigned umin2(unsigned a, unsigned b) { return a < b ? a : b; }
DI unsigned umax2(unsigned a, unsigned b) { return a > b ? a : b; }
DI unsigned umax4(u32x4 v) { return umax2(umax2(v[0], v[1]), umax2(v[2], v[3])); }
DI bool has_poison(u32x4 v) { return __builtin_amdgcn_ballot_w64(umax4(v) == POISON) != 0ull; }
DI u32x4 clean(u32x4 v) { return u32x4{umin2(v[0], POISON - 1u), umin2(v[1], POISON - 1u), umin2(v[2], POISON - 1u), umin2(v[3], POISON - 1u)}; }
DI void lds_wait(LAS unsigned* p, unsigned want) {
#pragma unroll 1
  for (unsigned spins = 0; spins < (SPIN_CAP << 6); ++spins) {
    if ((unsigned)__builtin_amdgcn_readfirstlane(__hip_atomic_load(p, RLX_WG)) >= want) break;
    __builtin_amdgcn_s_sleep(1);
  }
  asm volatile("" ::: "memory");
}
#define STAMP(i) do { } while (0)

__global__ void __launch_bounds__(576, 3) lin_kernel(const char* __restrict__ q, const char* __restrict__ k, const char* __restrict__ v,
                                                     const char* __restrict__ e, const char* __restrict__ f, char* ws,
                                                     char* __restrict__ out) {
  __shared__ __attribute__((aligned(1024))) char smem[L_BYTES];
  LAS char* L = (LAS char*)smem;
  LAS unsigned* flag = (LAS unsigned*)(L + L_FLAG);
  STAMP(0);
  const int b = blockIdx.x, xcd = b & 7, jj = b >> 3, head = xcd * 4 + (jj >> 3), sub = jj & 7;
  const int tensor = sub >> 2, role = sub & 3;
  const int tid = threadIdx.x, lane = tid & 63, w = __builtin_amdgcn_readfirstlane(tid >> 6);
  const bool helper = role == 3;
  const int lw = w & 3;
  const unsigned ldsb = (unsigned)(size_t)L;
  const __amdgpu_buffer_rsrc_t rsP = __builtin_amdgcn_make_buffer_rsrc(ws + (size_t)head * PAY_HEAD, 0, PAY_HEAD, 0x00020000);
  gu32* ctl = (gu32*)(ws + CTL_OFF + head * CTL_HEAD);

  const __amdgpu_buffer_rsrc_t rsQ =
      __builtin_amdgcn_make_buffer_rsrc((char*)q + ((size_t)head * SQ + sub * 256) * ROWB, 0, 256 * ROWB, 0x00020000);
  unsigned voQ[6];
  auto qprep = [&]() {
#pragma unroll
    for (int bb = 0; bb < 3; ++bb) {
      const int idx = 64 * bb + lane, row = idx / 24, c = idx - row * 24;
      voQ[bb] = row * 384 + (((c & 24) | ((c & 7) ^ (row >> 1))) << 4);
      voQ[3 + bb] = (voQ[bb] ^ 64u) + 3072u;
    }
    asm volatile("" : "+v"(voQ[0]), "+v"(voQ[1]), "+v"(voQ[2]), "+v"(voQ[3]), "+v"(voQ[4]), "+v"(voQ[5]));
  };
  auto qgroup = [&](int j) {
    const unsigned d = ldsb + (j < 8 ? L_SCR_LO : L_SCR_HI - 49152) + j * 6144;
#pragma unroll
    for (int i = 0; i < 6; ++i) dma_nt(d + i * 1024, voQ[i], rsQ, j * 6144);
  };

  if (w == 8) {
    gu32* inbox_p = ctl + (8 + sub * 8 + (lane & 7)) * 32;
    gu32* nonce_p = ctl + (lane & 7) * 32;
    gu32* ack_p = ctl + (8 + (lane & 7) * 8 + sub) * 32;
    const unsigned old = __hip_atomic_load(lane < 8 ? inbox_p : ctl + sub * 32, RLX_AGENT);
    if (lane < 8) flag[lane] = 0u;
    const u32x4 pz = {POISON, POISON, POISON, POISON};
    lgkm0();
    bars<1>();
    unsigned n = __builtin_amdgcn_readlane(old, 16) + 1u;
    while (__builtin_amdgcn_ballot_w64(old == n) != 0ull) ++n;
    if (lane < 3) __hip_atomic_store(lane == 0 ? ctl + sub * 32 : ctl + CNT_W + (sub * 2 + lane - 1) * 32, lane == 0 ? n : 0u, RLX_AGENT);
#pragma unroll 1
    for (int s = 0; s < 3; ++s) {
      if (helper || s == 0) {
        const unsigned base = lane * 16 + tensor * 24576 + (helper ? PART_OFF + s * 4096 : role * 4096);
#pragma unroll
        for (int i = 0; i < 8; ++i) st_sc1(pz, rsP, base + (i >> 2) * 12288 + (i & 3) * 1024);
      }
      bars<1>();
    }
    bars<2>();
    unsigned echoed = __hip_atomic_load(nonce_p, RLX_AGENT);
    bars<1>();
    vmcnt<1>();
    bars<3>();
    vmcnt<0>();
    if (lane < 8) __hip_atomic_store(ack_p, echoed, RLX_AGENT);
    bars<4>();
    unsigned inbox = __hip_atomic_load(inbox_p, RLX_AGENT);
    bars<4>();
    vmcnt<0>();
    const bool fast = __builtin_amdgcn_ballot_w64(inbox != n) == 0ull;
    unsigned cur = __hip_atomic_load(nonce_p, RLX_AGENT);
    bars<3>();
    if (lane < 8 && cur != echoed) __hip_atomic_store(ack_p, cur, RLX_AGENT);
    echoed = cur;
    if (fast && lane == 0) flag[F_HS] = 1u;
    lgkm0();
    qprep();
    bars<NST - 1 - 21>();
    int j = 0;
#pragma unroll 1
    for (; j < QPRE; ++j) qgroup(j);
    bars<1>();
#pragma unroll 1
    for (; j < 2 * QPRE; ++j) qgroup(j);
    if (!fast) {
#pragma unroll 1
      for (unsigned spins = 0; spins < SPIN_CAP; ++spins) {
        inbox = __hip_atomic_load(inbox_p, RLX_AGENT);
        vmcnt<0>();
        cur = __hip_atomic_load(nonce_p, RLX_AGENT);
        if (lane < 8 && cur != echoed) __hip_atomic_store(ack_p, cur, RLX_AGENT);
        echoed = cur;
        if (__builtin_amdgcn_ballot_w64(inbox != n) == 0ull) break;
        __builtin_amdgcn_s_sleep(2);
      }
      if (lane == 0) __hip_atomic_store(flag + F_HS, 1u, RLX_WG);
    }
    bars<1>();
    STAMP(1);
#pragma unroll 1
    for (; j < QHOLD; ++j) qgroup(j);
    STAMP(2);
    if (QHOLD < 8) {
      lds_wait(flag + F_ISS, 4u);
#pragma unroll 1
      for (; j < 8; ++j) qgroup(j);
    }
    STAMP(3);
    vmcnt<36>();
    if (lane == 0) __hip_atomic_store(flag + F_Q, 2u, RLX_WG);
    vmcnt<24>();
    if (lane == 0) __hip_atomic_store(flag + F_Q, 4u, RLX_WG);
    vmcnt<12>();
    if (lane == 0) __hip_atomic_store(flag + F_Q, 6u, RLX_WG);
    vmcnt<0>();
    if (lane == 0) __hip_atomic_store(flag + F_Q, 8u, RLX_WG);
    return;
  }

  const __amdgpu_buffer_rsrc_t rsK =
      __builtin_amdgcn_make_buffer_rsrc((char*)(tensor ? v : k) + (size_t)head * SKV * ROWB, 0, SKV * ROWB, 0x00020000);
  const __amdgpu_buffer_rsrc_t rsE = __builtin_amdgcn_make_buffer_rsrc((char*)(tensor ? f : e), 0, R * SKV * 2, 0x00020000);
  const unsigned rot = ROT == 1 ? (jj >> 3) * 6u : ROT == 2 ? ((jj >> 3) * 6u + role * 2u) : ROT == 3 ? (head * 5u) % 24u : 0u;
  unsigned sk = helper ? 1536u * ROWB : role * 128u + rot * 64u * ROWB, se = helper ? 3072u : rot * 128u;
  const unsigned wrap_e = helper ? 4096u : 3072u, back_e = helper ? 1024u : 3072u, back_k = helper ? 512u * ROWB - 128u : 1536u * ROWB;
  unsigned voK[2], voE[2];
#pragma unroll
  for (int x = 0; x < 2; ++x) {
    const int row = 8 * (2 * lw + x) + (lane >> 3);
    voK[x] = row * ROWB + (((lane & 7) ^ ((((lane >> 4) & 1) | (x << 1)) << 1)) << 4);
    voE[x] = row * (SKV * 2) + (((lane & 7) ^ ((x << 2) | (lane >> 4))) << 4);
  }
  unsigned dsl = ldsb + lw * 2048;
  auto issue = [&]() {
    dma_nt(dsl, voK[0], rsK, sk);
    dma_nt(dsl + 1024, voK[1], rsK, sk);
    dma(dsl + 8192, voE[0], rsE, se);
    dma(dsl + 9216, voE[1], rsE, se);
    sk += 64u * ROWB;
    se += 128u;
    if (se == wrap_e) { se -= back_e; sk -= back_k; }
    dsl += SLOT;
    if (dsl >= ldsb + RING * SLOT) dsl -= RING * SLOT;
  };
  if (w >= 4) {
#pragma unroll
    for (int s = 0; s < RING - 1; ++s) issue();
  }
  const int g = lane >> 4, q4 = (lane & 15) >> 2, p4 = lane & 3, r16 = lane & 15;
  const int r32 = lane & 31, hh = lane >> 5;
  const int rt16 = w & 3, ch = w >> 2;
  const unsigned fin_v = tensor ? 24576 + ch * 12288 + rt16 * 1024 + lane * 16
                                : (((rt16 >> 1) * 12 + 2 * ch + (g & 1)) * 64 + (g >> 1) * 32 + (rt16 & 1) * 16 + r16) * 16;
  const int fk = (q4 >> 1) | ((g & 1) << 1);
  const unsigned kt0 = (8 * g + q4) * 128 + (((2 * ch) ^ fk) << 5) + p4 * 8;
  const unsigned kt1 = (8 * g + q4) * 128 + (((2 * ch + 1) ^ fk) << 5) + p4 * 8;
  const unsigned et0 = 8192 + (16 * rt16 + r16) * 128 + ((g ^ (r16 >> 1)) << 4);
  const unsigned et1 = 8192 + (16 * rt16 + r16) * 128 + (((4 + g) ^ (r16 >> 1)) << 4);
  const unsigned go = lw * 1024 + lane * 16, gs = ch * 24576;
  unsigned dst[6];
#pragma unroll
  for (int i = 0; i < 6; ++i) {
    const unsigned d = 64 * (i % 3) + 32 * (i / 3) + 16 * hh + r16, d6 = d / 6u;
    dst[i] = ch ? L_VP + (d - 6 * d6) * VT + lw * 1024 + (g & 1) * 512 + d6 * 16 : L_KP + i * 4096 + go;
  }
  gu32* cnt_p = ctl + CNT_W + (w == 1 ? (lane & 7) * 2 + tensor : sub * 2 + ch) * 32;
  asm volatile("" : "+v"(dst[0]), "+v"(dst[1]), "+v"(dst[2]), "+v"(dst[3]), "+v"(dst[4]), "+v"(dst[5]), "+v"(cnt_p));
  qprep();
  f32x4 acc0 = {0.f, 0.f, 0.f, 0.f}, acc1 = {0.f, 0.f, 0.f, 0.f};
  int so = 0;
  auto compute = [&]() {
    LAS char* sl = L + so;
    const u32x4 a00 = tr2(sl + kt0, sl + kt0 + 512), a01 = tr2(sl + kt1, sl + kt1 + 512);
    const u32x4 b0 = *(LAS u32x4*)(sl + et0);
    const u32x4 a10 = tr2(sl + kt0 + 4096, sl + kt0 + 4608), a11 = tr2(sl + kt1 + 4096, sl + kt1 + 4608);
    const u32x4 b1 = *(LAS u32x4*)(sl + et1);
    if (tensor) {
      acc0 = mfma16(b0, a00, acc0);
      acc1 = mfma16(b0, a01, acc1);
      acc0 = mfma16(b1, a10, acc0);
      acc1 = mfma16(b1, a11, acc1);
    } else {
      acc0 = mfma16(a00, b0, acc0);
      acc1 = mfma16(a01, b0, acc1);
      acc0 = mfma16(a10, b1, acc0);
      acc1 = mfma16(a11, b1, acc1);
    }
    so += SLOT;
    if (so == RING * SLOT) so = 0;
  };
  auto cross = [&](u32x4 x) {
    if (tensor) {
      auto r0 = __builtin_amdgcn_permlane32_swap(x[0], x[2], false, false);
      auto r1 = __builtin_amdgcn_permlane32_swap(x[1], x[3], false, false);
      return u32x4{r0[0], r1[0], r0[1], r1[1]};
    }
    auto r0 = __builtin_amdgcn_permlane16_swap(x[0], x[2], false, false);
    auto r1 = __builtin_amdgcn_permlane16_swap(x[1], x[3], false, false);
    return u32x4{r0[0], r1[0], r0[1], r1[1]};
  };
  auto publish = [&](unsigned off) {
    st_sc1(clean(cross(u32x4{pk2(acc0[0], acc0[1]), pk2(acc0[2], acc0[3]), pk2(acc1[0], acc1[1]), pk2(acc1[2], acc1[3])})), rsP, fin_v + off);
  };
#define STAGE_HEAD(REM, XTRA)                                                  \
  do {                                                                         \
    if (w >= 4) vmcnt<4 * ((REM) < RING - 2 ? (REM) : RING - 2) + (XTRA)>();   \
    bar();                                                                     \
    if ((REM) >= RING - 1 && w >= 4) issue();                                  \
  } while (0)
#define STAGE(REM, XTRA) do { STAGE_HEAD(REM, XTRA); compute(); } while (0)
  static_assert(RING >= 4 && RING <= 6, "the waits below are written for this range");
#pragma unroll 1
  for (int s = 0; s < NST - 6; ++s) {
    STAGE(6, 0);
    if (helper && (s & 7) == 7) {
      publish(PART_OFF + (s >> 3) * 4096);
      acc0 = f32x4{0.f, 0.f, 0.f, 0.f};
      acc1 = f32x4{0.f, 0.f, 0.f, 0.f};
    }
  }
  STAGE(5, 0);
  STAGE(4, 0);
  STAGE(3, 0);
  STAGE_HEAD(2, 0);
  const unsigned hs_seen = __hip_atomic_load(flag + F_HS, RLX_WG);
  const unsigned part_c = role < 2 ? PART_OFF + role * 4096 : PAY_HEAD;
  u32x4 pa = ld_sc1(rsP, fin_v, part_c);
  compute();
  STAGE(1, 1);
  STAGE(0, 1);
  STAMP(3);
  if (__builtin_amdgcn_readfirstlane(hs_seen) == 0u) {
    lds_wait(flag + F_HS, 1u);
    pa[0] = POISON;
  }
#pragma unroll 1
  for (unsigned spins = 0; spins < SPIN_CAP; ++spins) {
    if (!has_poison(pa)) break;
    __builtin_amdgcn_s_sleep(8);
    asm volatile("" ::: "memory");
    pa = ld_sc1(rsP, fin_v, part_c);
  }
  pa = cross(pa);
  acc0 += f32x4{__uint_as_float(pa[0] << 16), __uint_as_float(pa[0] & 0xffff0000u), __uint_as_float(pa[1] << 16), __uint_as_float(pa[1] & 0xffff0000u)};
  acc1 += f32x4{__uint_as_float(pa[2] << 16), __uint_as_float(pa[2] & 0xffff0000u), __uint_as_float(pa[3] << 16), __uint_as_float(pa[3] & 0xffff0000u)};
  publish(helper ? PART_OFF + 8192 : role * 4096);
  asm volatile("" ::: "memory");
  bar();
  STAMP(2);
  {
    if (w == 1 && lane < 8) __hip_atomic_fetch_add(cnt_p, 1u, RLX_AGENT);
    if (lw == 0) {
      if (lane == 0) {
        unsigned c0, c1, c2;
        const unsigned zero = 0u;
        asm volatile(
            "global_atomic_add %0, %3, %4, off sc0\n\ts_sleep %5\n\t"
            "global_atomic_add %1, %3, %4, off sc0\n\ts_sleep %5\n\t"
            "s_mov_b32 s4, 0x100000\n"
            "1:\n\t"
            "global_atomic_add %2, %3, %4, off sc0\n\ts_sleep %5\n\t"
            "s_waitcnt vmcnt(2)\n\tv_cmp_lt_u32 vcc, 3, %0\n\ts_cbranch_vccnz 2f\n\t"
            "global_atomic_add %0, %3, %4, off sc0\n\ts_sleep %5\n\t"
            "s_waitcnt vmcnt(2)\n\tv_cmp_lt_u32 vcc, 3, %1\n\ts_cbranch_vccnz 2f\n\t"
            "global_atomic_add %1, %3, %4, off sc0\n\ts_sleep %5\n\t"
            "s_waitcnt vmcnt(2)\n\tv_cmp_lt_u32 vcc, 3, %2\n\ts_cbranch_vccnz 2f\n\t"
            "s_sub_u32 s4, s4, 1\n\ts_cbranch_scc0 1b\n"
            "2:\n\t"
            "ds_write_b32 %6, %7\n\t"
            "s_waitcnt vmcnt(0)"
            : "=&v"(c0), "=&v"(c1), "=&v"(c2)
            : "v"(cnt_p), "v"(zero), "n"(POLLGAP), "v"(ldsb + L_FLAG + (F_GO + ch) * 4), "v"(1u)
            : "memory", "vcc", "scc", "s4");
      }
    } else {
      lds_wait(flag + F_GO + ch, 1u);
    }
    STAMP(11);
    unsigned pend = 7u;
    u32x4 gv[8];
#pragma unroll
    for (int i = 0; i < 8; ++i)
      asm volatile("buffer_load_dwordx4 %0, %1, %2, %3 offen sc1"
                   : "=v"(gv[i]) : "v"(go), "s"(rsP), "s"(gs + (i < 6 ? i * 4096 : PART_OFF + (i == 6 ? 2 : 5) * 4096)) : "memory");
    if (ch) {
      qgroup(2 * w);
      qgroup(2 * w + 1);
      vmcnt<12>();
    } else {
      if (lane == 0) __hip_atomic_fetch_add(flag + F_ISS, 1u, RLX_WG);
      vmcnt<0>();
    }
#pragma unroll 1
    for (unsigned spins = 0; spins < SPIN_CAP; ++spins) {
      asm volatile("" : "+v"(gv[0]), "+v"(gv[1]), "+v"(gv[2]), "+v"(gv[3]), "+v"(gv[4]), "+v"(gv[5]), "+v"(gv[6]), "+v"(gv[7])::"memory");
#pragma unroll
      for (int c = 0; c < 2; ++c) {
        if (((pend >> c) & 1u) && __builtin_amdgcn_ballot_w64(umax2(umax4(gv[c]), umax4(gv[3 + c])) == POISON) == 0ull) {
          *(LAS u32x4*)(L + dst[c]) = gv[c];
          *(LAS u32x4*)(L + dst[3 + c]) = gv[3 + c];
          pend &= ~(1u << c);
        }
      }
      if ((pend & 4u) && __builtin_amdgcn_ballot_w64(umax2(umax2(umax4(gv[2]), umax4(gv[5])), umax2(umax4(gv[6]), umax4(gv[7]))) == POISON) == 0ull) {
        *(LAS u32x4*)(L + dst[2]) =
            u32x4{addbf2(gv[2][0], gv[6][0]), addbf2(gv[2][1], gv[6][1]), addbf2(gv[2][2], gv[6][2]), addbf2(gv[2][3], gv[6][3])};
        *(LAS u32x4*)(L + dst[5]) =
            u32x4{addbf2(gv[5][0], gv[7][0]), addbf2(gv[5][1], gv[7][1]), addbf2(gv[5][2], gv[7][2]), addbf2(gv[5][3], gv[7][3])};
        pend &= 3u;
      }
      if (pend == 0u) break;
      if (RETRY_SLEEP) __builtin_amdgcn_s_sleep(RETRY_SLEEP);
      asm volatile("" ::: "memory");
#pragma unroll
      for (int i = 0; i < 6; ++i) gv[i] = ld_sc1(rsP, go, (pend >> (i % 3)) & 1u ? gs + i * 4096 : PAY_HEAD);
      gv[6] = ld_sc1(rsP, go, pend & 4u ? gs + PART_OFF + 2 * 4096 : PAY_HEAD);
      gv[7] = ld_sc1(rsP, go, pend & 4u ? gs + PART_OFF + 5 * 4096 : PAY_HEAD);
      asm volatile("" : "+v"(gv[0]), "+v"(gv[1]), "+v"(gv[2]), "+v"(gv[3]), "+v"(gv[4]), "+v"(gv[5]), "+v"(gv[6]), "+v"(gv[7])::"memory");
    }
    lgkm0();
    if (lane == 0) __hip_atomic_fetch_add(flag + F_KP + ch, 1u, RLX_WG);
  }
  STAMP(4);
  LAS char* scr = L + (w < 4 ? L_SCR_LO : L_SCR_HI) + lw * 12288;
  unsigned fa[4];
#pragma unroll
  for (int k4 = 0; k4 < 4; ++k4) fa[k4] = r32 * 384 + (((2 * k4 + hh) ^ ((r32 >> 1) & 7)) << 4);
  if (ch) vmcnt<0>();
  else lds_wait(flag + F_Q, 2 * w + 2);
  STAMP(5);
  u32x4 qf[12];
#pragma unroll
  for (int ks = 0; ks < 12; ++ks) qf[ks] = *(LAS u32x4*)(scr + fa[ks & 3] + (ks >> 2) * 128);
  lds_wait(flag + F_KP, 4u);
  STAMP(6);

  f32x16 s0, s1;
#pragma unroll
  for (int i = 0; i < 16; ++i) { s0[i] = 0.f; s1[i] = 0.f; }
  {
    LAS u32x4* kf = (LAS u32x4*)(L + L_KP) + lane;
    u32x4 ka[24];
#pragma unroll
    for (int i = 0; i < 6; ++i) ka[i] = kf[i * 64];
    __builtin_amdgcn_sched_barrier(0);
#pragma unroll
    for (int i = 0; i < 24; ++i) {
      if (i + 6 < 24) ka[i + 6] = kf[(i + 6) * 64];
      if (i < 12) s0 = mfma32(ka[i], qf[i], s0);
      else s1 = mfma32(ka[i], qf[i - 12], s1);
      __builtin_amdgcn_sched_barrier(0);
    }
  }
  float m = s0[0];
#pragma unroll
  for (int i = 1; i < 16; ++i) m = fmaxf(m, s0[i]);
#pragma unroll
  for (int i = 0; i < 16; ++i) m = fmaxf(m, s1[i]);
  {
    auto rr = __builtin_amdgcn_permlane32_swap(__float_as_uint(m), __float_as_uint(m), false, false);
    m = fmaxf(__uint_as_float(rr[0]), __uint_as_float(rr[1]));
  }
  const float cs = 0.07216878364870322f * 1.4426950408889634f;
  const float mc = m * cs;
  float sum = 0.f;
#pragma unroll
  for (int i = 0; i < 16; ++i) { s0[i] = __builtin_amdgcn_exp2f(__builtin_fmaf(s0[i], cs, -mc)); sum += s0[i]; }
#pragma unroll
  for (int i = 0; i < 16; ++i) { s1[i] = __builtin_amdgcn_exp2f(__builtin_fmaf(s1[i], cs, -mc)); sum += s1[i]; }
  {
    auto rr = __builtin_amdgcn_permlane32_swap(__float_as_uint(sum), __float_as_uint(sum), false, false);
    sum = __uint_as_float(rr[0]) + __uint_as_float(rr[1]);
  }
  const float inv = 1.0f / sum;
  u32x4 pf[4];
#pragma unroll
  for (int t = 0; t < 2; ++t) {
    pf[t] = u32x4{pk2(s0[8 * t] * inv, s0[8 * t + 1] * inv), pk2(s0[8 * t + 2] * inv, s0[8 * t + 3] * inv),
                  pk2(s0[8 * t + 4] * inv, s0[8 * t + 5] * inv), pk2(s0[8 * t + 6] * inv, s0[8 * t + 7] * inv)};
    pf[2 + t] = u32x4{pk2(s1[8 * t] * inv, s1[8 * t + 1] * inv), pk2(s1[8 * t + 2] * inv, s1[8 * t + 3] * inv),
                      pk2(s1[8 * t + 4] * inv, s1[8 * t + 5] * inv), pk2(s1[8 * t + 6] * inv, s1[8 * t + 7] * inv)};
  }
  STAMP(7);
  lds_wait(flag + F_VP, 4u);
  STAMP(8);
  f32x16 o[6];
#pragma unroll
  for (int t = 0; t < 6; ++t)
#pragma unroll
    for (int i = 0; i < 16; ++i) o[t][i] = 0.f;
  {
    LAS char* vb = L + L_VP + lane * 16;
    u32x4 va[24];
#pragma unroll
    for (int i = 0; i < 6; ++i) va[i] = *(LAS u32x4*)(vb + (i % 6) * VT + (i / 6) * 1024);
    __builtin_amdgcn_sched_barrier(0);
#pragma unroll
    for (int i = 0; i < 24; ++i) {
      if (i + 6 < 24) va[i + 6] = *(LAS u32x4*)(vb + ((i + 6) % 6) * VT + ((i + 6) / 6) * 1024);
      o[i % 6] = mfma32(pf[i / 6], va[i], o[i % 6]);
      __builtin_amdgcn_sched_barrier(0);
    }
  }
  const __amdgpu_buffer_rsrc_t rsO =
      __builtin_amdgcn_make_buffer_rsrc(out + ((size_t)head * SQ + sub * 256 + w * 32) * ROWB, 0, 32 * ROWB, 0x00020000);
  const unsigned ov = hh * 4 * ROWB + r32 * 12;
#pragma unroll
  for (int j = 0; j < 16; ++j) {
    const u32x3 x = {pk2(o[0][j], o[1][j]), pk2(o[2][j], o[3][j]), pk2(o[4][j], o[5][j])};
    __builtin_amdgcn_raw_buffer_store_b96(x, rsO, ov + ((j & 3) + 8 * (j >> 2)) * ROWB, 0, 2);
  }
}

extern "C" void kernel_launch(void* const* d_in, const int* in_sizes, int n_in, void* d_out, int out_size, void* d_ws, size_t ws_size,
                              hipStream_t stream) {
  lin_kernel<<<256, 576, 0, stream>>>((const char*)d_in[0], (const char*)d_in[1], (const char*)d_in[2], (const char*)d_in[3],
                                      (const char*)d_in[4], (char*)d_ws, (char*)d_out);
}
```

```cpp
#include <hip/hip_runtime.h>
#include <hip/hip_bf16.h>

typedef short s16x4 __attribute__((ext_vector_type(4)));
typedef __bf16 bf16x8 __attribute__((ext_vector_type(8)));
typedef __bf16 bf16x2 __attribute__((ext_vector_type(2)));
typedef float f32x2 __attribute__((ext_vector_type(2)));
typedef float f32x4 __attribute__((ext_vector_type(4)));
typedef float f32x16 __attribute__((ext_vector_type(16)));
typedef unsigned u32x4 __attribute__((ext_vector_type(4)));
typedef unsigned u32x2 __attribute__((ext_vector_type(2)));
typedef unsigned u32x3 __attribute__((ext_vector_type(3)));
#define LAS __attribute__((address_space(3)))
#define GAS __attribute__((address_space(1)))
#define DI __device__ __forceinline__
#define RLX_AGENT __ATOMIC_RELAXED, __HIP_MEMORY_SCOPE_AGENT
#define RLX_WG __ATOMIC_RELAXED, __HIP_MEMORY_SCOPE_WORKGROUP
typedef GAS unsigned gu32;

#define RING 5
#define QPRE 2
#define POLLGAP 3
#define RETRY_SLEEP 32
#define W1 36
#define W2 24
#define S2START 4
#define ISSEARLY 1
#define QSTART 2
#define NDELAY 0
#define GDELAY 0
#define NLINES 0
#define R0AT 0
#define NOMID 0
#define STAMPS 0

constexpr int H = 32, SQ = 2048, SKV = 2048, DH = 192, R = 64;
constexpr int ROWB = DH * 2;
constexpr int SLOT = 16384;
constexpr int NST = 24;
constexpr int PART_OFF = 49152;
constexpr int PAY_HEAD = 2 * 49152;
constexpr int CTL_OFF = H * PAY_HEAD;
constexpr int CTL_HEAD = 33 * 128;
constexpr int DBG_OFF = CTL_OFF + H * CTL_HEAD + 4096;
constexpr unsigned POISON = 0xFFFFFFFFu;
constexpr unsigned SPIN_CAP = 1u << 20;
constexpr int VT = 4096 + 16;
constexpr int L_KP = 0, L_VP = 24576, L_SCR_HI = 49152 + 256, L_SCR_LO = L_SCR_HI + 49152, L_FLAG = L_SCR_LO + 49152, L_BYTES = L_FLAG + 64;
constexpr int F_HS = 0, F_KP = 1, F_VP = 2, F_Q = 3, F_ISS = 4, F_GO = 5  , F_Q2 = 7;

template <bool B> struct BoolC { static constexpr bool value = B; };
DI unsigned pk2(float a, float b) {
  f32x2 f = {a, b};
  return __builtin_bit_cast(unsigned, __builtin_convertvector(f, bf16x2));
}
DI f32x4 mfma16(u32x4 a, u32x4 b, f32x4 c) {
  return __builtin_amdgcn_mfma_f32_16x16x32_bf16(__builtin_bit_cast(bf16x8, a), __builtin_bit_cast(bf16x8, b), c, 0, 0, 0);
}
DI f32x16 mfma32(u32x4 a, u32x4 b, f32x16 c) {
  return __builtin_amdgcn_mfma_f32_32x32x16_bf16(__builtin_bit_cast(bf16x8, a), __builtin_bit_cast(bf16x8, b), c, 0, 0, 0);
}
DI u32x4 tr2(LAS char* p0, LAS char* p1) {
  u32x2 l2 = __builtin_bit_cast(u32x2, __builtin_amdgcn_ds_read_tr16_b64_v4i16((LAS s16x4*)p0));
  u32x2 h2 = __builtin_bit_cast(u32x2, __builtin_amdgcn_ds_read_tr16_b64_v4i16((LAS s16x4*)p1));
  return u32x4{l2[0], l2[1], h2[0], h2[1]};
}
DI void dma_nt(unsigned lds, unsigned voff, __amdgpu_buffer_rsrc_t rs, unsigned soff) {
  unsigned keep;
  asm volatile("s_mov_b32 %0, m0\n\ts_mov_b32 m0, %1\n\ts_nop 0\n\tbuffer_load_dwordx4 %2, %3, %4 offen nt lds\n\ts_mov_b32 m0, %0"
               : "=&s"(keep) : "s"(lds), "v"(voff), "s"(rs), "s"(soff) : "memory");
}
DI void dma(unsigned lds, unsigned voff, __amdgpu_buffer_rsrc_t rs, unsigned soff) {
  unsigned keep;
  asm volatile("s_mov_b32 %0, m0\n\ts_mov_b32 m0, %1\n\ts_nop 0\n\tbuffer_load_dwordx4 %2, %3, %4 offen lds\n\ts_mov_b32 m0, %0"
               : "=&s"(keep) : "s"(lds), "v"(voff), "s"(rs), "s"(soff) : "memory");
}
template <int N> DI void vmcnt() { asm volatile("s_waitcnt vmcnt(%0)" ::"n"(N) : "memory"); }
DI void lgkm0() { asm volatile("s_waitcnt lgkmcnt(0)" ::: "memory"); }
DI void bar() {
  lgkm0();
  __builtin_amdgcn_s_barrier();
  asm volatile("" ::: "memory");
}
template <int N> DI void bars() {
#pragma unroll
  for (int i = 0; i < N; ++i) __builtin_amdgcn_s_barrier();
  asm volatile("" ::: "memory");
}
DI u32x4 ld_sc1(__amdgpu_buffer_rsrc_t rs, unsigned voff, unsigned soff) { return __builtin_amdgcn_raw_buffer_load_b128(rs, voff, soff, 16); }
DI void st_sc1(u32x4 v, __amdgpu_buffer_rsrc_t rs, unsigned voff) { __builtin_amdgcn_raw_buffer_store_b128(v, rs, voff, 0, 16); }
DI unsigned addbf2(unsigned x, unsigned y) {
  const float lo = __uint_as_float(x << 16) + __uint_as_float(y << 16);
  const float hi = __uint_as_float(x & 0xffff0000u) + __uint_as_float(y & 0xffff0000u);
  return pk2(lo, hi);
}
DI unsigned umin2(unsigned a, unsigned b) { return a < b ? a : b; }
DI unsigned umax2(unsigned a, unsigned b) { return a > b ? a : b; }
DI unsigned umax4(u32x4 v) { return umax2(umax2(v[0], v[1]), umax2(v[2], v[3])); }
DI bool has_poison(u32x4 v) { return __builtin_amdgcn_ballot_w64(umax4(v) == POISON) != 0ull; }
DI u32x4 clean(u32x4 v) { return u32x4{umin2(v[0], POISON - 1u), umin2(v[1], POISON - 1u), umin2(v[2], POISON - 1u), umin2(v[3], POISON - 1u)}; }
DI unsigned at_read(gu32* p) {
  unsigned z = 0u;
  asm volatile("" : "+v"(z));
  return __hip_atomic_fetch_add(p, z, RLX_AGENT);
}
DI void at_write(gu32* p, unsigned v) { asm volatile("global_atomic_swap %0, %1, off" ::"v"(p), "v"(v) : "memory"); }
DI void lds_wait(LAS unsigned* p, unsigned want) {
#pragma unroll 1
  for (unsigned spins = 0; spins < (SPIN_CAP << 6); ++spins) {
    if ((unsigned)__builtin_amdgcn_readfirstlane(__hip_atomic_load(p, RLX_WG)) >= want) break;
    __builtin_amdgcn_s_sleep(1);
  }
  asm volatile("" ::: "memory");
}
#define STAMP(i) do { } while (0)

__global__ void __launch_bounds__(704, 3) lin_kernel(const char* __restrict__ q, const char* __restrict__ k, const char* __restrict__ v,
                                                     const char* __restrict__ e, const char* __restrict__ f, char* ws,
                                                     char* __restrict__ out) {
  __shared__ __attribute__((aligned(1024))) char smem[L_BYTES];
  LAS char* L = (LAS char*)smem;
  LAS unsigned* flag = (LAS unsigned*)(L + L_FLAG);
  STAMP(0);
  const int b = blockIdx.x, xcd = b & 7, jj = b >> 3, head = xcd * 4 + (jj >> 3), sub = jj & 7;
  const int tensor = sub >> 2, role = sub & 3;
  const int tid = threadIdx.x, lane = tid & 63, w = __builtin_amdgcn_readfirstlane(tid >> 6);
  const bool helper = role == 3;
  const int lw = w & 3;
  const unsigned ldsb = (unsigned)(size_t)L;
  const __amdgpu_buffer_rsrc_t rsK =
      __builtin_amdgcn_make_buffer_rsrc((char*)(tensor ? v : k) + (size_t)head * SKV * ROWB, 0, SKV * ROWB, 0x00020000);
  const __amdgpu_buffer_rsrc_t rsE = __builtin_amdgcn_make_buffer_rsrc((char*)(tensor ? f : e), 0, R * SKV * 2, 0x00020000);
  unsigned sk = helper ? 1536u * ROWB : role * 128u, se = helper ? 3072u : 0u;
  int pairs = helper ? 16 : 0;
  unsigned voK[2], voE[2];
#pragma unroll
  for (int x = 0; x < 2; ++x) {
    const int row = 8 * (2 * lw + x) + (lane >> 3);
    voK[x] = row * ROWB + (((lane & 7) ^ (((lane >> 4) & 1) << 2)) << 4);
    voE[x] = row * (SKV * 2) + (((lane & 7) ^ ((x << 2) | (lane >> 4))) << 4);
  }
  unsigned dsl = ldsb + lw * 2048;
  auto issue = [&]() {
    dma_nt(dsl, voK[0], rsK, sk);
    dma_nt(dsl + 1024, voK[1], rsK, sk);
    dma(dsl + 8192, voE[0], rsE, se);
    dma(dsl + 9216, voE[1], rsE, se);
    if (pairs > 0) {
      --pairs;
      sk += pairs & 1 ? 128u : pairs ? 64u * ROWB - 128u : 128u - 448u * ROWB;
      se += pairs & 1 ? 0u : pairs ? 128u : -896u;
    } else {
      sk += 64u * ROWB;
      se += 128u;
    }
    dsl += SLOT;
    if (dsl >= ldsb + RING * SLOT) dsl -= RING * SLOT;
  };
  if ((w & 12) == 4) {
#pragma unroll
    for (int s = 0; s < RING - 1; ++s) issue();
  }
  __builtin_amdgcn_sched_barrier(0);
  const __amdgpu_buffer_rsrc_t rsP = __builtin_amdgcn_make_buffer_rsrc(ws + (size_t)head * PAY_HEAD, 0, PAY_HEAD, 0x00020000);
  gu32* ctl = (gu32*)(ws + CTL_OFF + head * CTL_HEAD);

  const __amdgpu_buffer_rsrc_t rsQ =
      __builtin_amdgcn_make_buffer_rsrc((char*)q + ((size_t)head * SQ + sub * 256) * ROWB, 0, 256 * ROWB, 0x00020000);
  unsigned voQ[6];
  auto qprep = [&]() {
#pragma unroll
    for (int bb = 0; bb < 3; ++bb) {
      const int idx = 64 * bb + lane, row = idx / 24, c = idx - row * 24;
      voQ[bb] = row * 384 + (((c & 24) | ((c & 7) ^ (row >> 1))) << 4);
      voQ[3 + bb] = (voQ[bb] ^ 64u) + 3072u;
    }
    asm volatile("" : "+v"(voQ[0]), "+v"(voQ[1]), "+v"(voQ[2]), "+v"(voQ[3]), "+v"(voQ[4]), "+v"(voQ[5]));
  };
  auto qgroup = [&](int j) {
    const unsigned d = ldsb + (j < 8 ? L_SCR_LO : L_SCR_HI - 49152) + j * 6144;
#pragma unroll
    for (int i = 0; i < 6; ++i) dma_nt(d + i * 1024, voQ[i], rsQ, j * 6144);
  };

  if (__builtin_expect(w == 8, 0)) {
    const int l8 = lane & 7;
    gu32* nonce_p = NLINES ? ctl + (25 + l8) * 32 : ctl + l8;
    gu32* inbox_p = ctl + (1 + 2 * sub + (l8 >> 2)) * 32 + (l8 & 3);
    gu32* note_p = ctl + (1 + 2 * l8 + tensor) * 32 + role;
    gu32* early_p = ctl + (17 + sub) * 32;
    gu32* mine_p = NLINES ? ctl + (25 + sub) * 32 : ctl + sub;
    unsigned old = 0u;
    if (R0AT) {
      if (lane < 10) old = at_read(lane < 8 ? inbox_p : lane == 8 ? mine_p : early_p);
    } else {
      old = __hip_atomic_load(lane < 8 ? inbox_p : lane == 8 ? mine_p : early_p, RLX_AGENT);
    }
    if (lane < 8) flag[lane] = lane == F_HS && role >= 2;
    const u32x4 pz = {POISON, POISON, POISON, POISON};
#pragma unroll 1
    for (int s = helper ? 0 : 2; s < 3; ++s) {
      const unsigned base = lane * 16 + tensor * 24576 + (helper ? PART_OFF + s * 4096 : role * 4096);
#pragma unroll
      for (int i = 0; i < 8; ++i) st_sc1(pz, rsP, base + (i >> 2) * 12288 + (i & 3) * 1024);
    }
    lgkm0();
    bars<1>();
    unsigned n = __builtin_amdgcn_readlane(old, 8) + 1u;
    while (__builtin_amdgcn_ballot_w64(old == n) != 0ull) ++n;
    if (lane == 0) at_write(mine_p, n);
    bars<7>();
    unsigned echoed = __hip_atomic_load(nonce_p, RLX_AGENT);
    bars<5>();
    vmcnt<0>();
    asm volatile("" : "+v"(echoed));
    if (helper && lane < 8 && (lane >> 1) == tensor * 2) at_write(ctl + (17 + lane) * 32, echoed);
    bars<4>();
    unsigned early = ~n;
    if (role < 2) early = __hip_atomic_load(early_p, RLX_AGENT);
    bars<4>();
    asm volatile("" : "+v"(early));
    bool trusted = role >= 2 || __builtin_amdgcn_readfirstlane(early) == n;
    if (trusted && lane == 0) flag[F_HS] = 1u;
    lgkm0();
    bars<3>();
    if (NDELAY) __builtin_amdgcn_s_sleep(NDELAY);
    if (lane < 8) at_write(note_p, echoed);
    auto refresh = [&]() {
      const unsigned cur = __hip_atomic_load(nonce_p, RLX_AGENT);
      if (lane < 8 && cur != echoed) at_write(note_p, cur);
      echoed = cur;
    };
#pragma unroll 1
    for (unsigned spins = 0; !trusted && spins < SPIN_CAP; ++spins) {
      unsigned x = 0u;
      if (lane < 2) x = at_read(lane ? early_p : ctl + (1 + 2 * sub + tensor) * 32 + 3);
      trusted = (__builtin_amdgcn_ballot_w64(x == n) & 3ull) != 0ull;
      if (trusted) break;
      refresh();
    }
    if (lane == 0) __hip_atomic_store(flag + F_HS, 1u, RLX_WG);
    bars<1>();
    unsigned seen = 0u;
#pragma unroll 1
    for (unsigned spins = 0; spins < (SPIN_CAP >> 3); ++spins) {
      if (lane < 8) {
        unsigned c0, c1, c2;
#define POLL_STEP(NEW, OLD)                                                                                                        \
  "global_atomic_add " NEW ", %4, %5, off sc0\n\ts_sleep %6\n\ts_waitcnt vmcnt(2)\n\tv_cmp_eq_u32 vcc, %9, " OLD "\n\t"           \
  "s_or_b32 %3, %3, vcc_lo\n\tv_and_b32 " OLD ", %3, %10\n\tv_cmp_eq_u32 vcc, " OLD ", %10\n\t"                                  \
  "s_and_saveexec_b64 s[6:7], vcc\n\tds_write_b32 %7, %8\n\ts_mov_b64 exec, s[6:7]\n\ts_cmp_eq_u32 %3, 0xff\n\ts_cbranch_scc1 2f\n\t"
        asm volatile(
            "global_atomic_add %0, %4, %5, off sc0\n\ts_sleep %6\n\t"
            "global_atomic_add %1, %4, %5, off sc0\n\ts_sleep %6\n\t"
            "s_mov_b32 s4, 7\n"
            "1:\n\t" POLL_STEP("%2", "%0") POLL_STEP("%0", "%1") POLL_STEP("%1", "%2")
            "s_sub_u32 s4, s4, 1\n\ts_cbranch_scc0 1b\n"
            "2:\n\t"
            "s_waitcnt vmcnt(0) lgkmcnt(0)"
            : "=&v"(c0), "=&v"(c1), "=&v"(c2), "+s"(seen)
            : "v"(inbox_p), "v"(0u), "n"(POLLGAP), "v"(ldsb + L_FLAG + (F_GO + (l8 >> 2)) * 4), "v"(1u), "s"(n), "v"(l8 & 4 ? 0xf0u : 0x0fu)
            : "memory", "vcc", "scc", "s4", "s6", "s7");
      }
      seen = __builtin_amdgcn_readfirstlane(seen);
      refresh();
      if (seen == 0xffu) break;
    }
    if (lane < 2) __hip_atomic_store(flag + F_GO + lane, 1u, RLX_WG);
    return;
  }
  if (__builtin_expect(w == 9, 0)) {
    qprep();
    bars<NST + 1 - QSTART>();
    int j = 0;
#pragma unroll 1
    for (int i = 0; i < QSTART; ++i) {
#pragma unroll 1
      for (int x = 0; x < QPRE && j < 8; ++x, ++j) qgroup(j);
      bars<1>();
    }
#pragma unroll 1
    for (; j < 8; ++j) {
      if ((unsigned)__builtin_amdgcn_readfirstlane(__hip_atomic_load(flag + F_ISS, RLX_WG)) < 8u) {
        vmcnt<W1 - 6>();
        if (j + 1 - W1 / 6 > 0 && lane == 0) __hip_atomic_store(flag + F_Q, (unsigned)(j + 1 - W1 / 6), RLX_WG);
      }
      qgroup(j);
    }
#define QTAIL(F, N)                                                        \
  vmcnt<6 * (N)>();                                                        \
  if (lane == 0) __hip_atomic_store(flag + (F), 8u - (N), RLX_WG);
    vmcnt<36>();
    if (lane == 0) __hip_atomic_fetch_max(flag + F_Q, 2u, RLX_WG);
    vmcnt<24>();
    if (lane == 0) __hip_atomic_fetch_max(flag + F_Q, 4u, RLX_WG);
    QTAIL(F_Q, 3) QTAIL(F_Q, 2) QTAIL(F_Q, 1) QTAIL(F_Q, 0)
    return;
  }
  if (__builtin_expect(w == 10, 0)) {
    qprep();
    bars<NST + 1>();
    STAMP(1);
    bool wide = false;
#pragma unroll 1
    for (unsigned spins = 0; spins < (SPIN_CAP << 6); ++spins) {
      wide = (unsigned)__builtin_amdgcn_readfirstlane(__hip_atomic_load(flag + F_ISS, RLX_WG)) >= 8u;
      if (wide || (unsigned)__builtin_amdgcn_readfirstlane(__hip_atomic_load(flag + F_Q, RLX_WG)) >= S2START) break;
      __builtin_amdgcn_s_sleep(1);
    }
    STAMP(2);
#pragma unroll 1
    for (int j = 8; j < 16; ++j) {
      if (!wide) {
        vmcnt<W2 - 6>();
        wide = (unsigned)__builtin_amdgcn_readfirstlane(__hip_atomic_load(flag + F_ISS, RLX_WG)) >= 8u;
      }
      qgroup(j);
    }
    STAMP(6);
    QTAIL(F_Q2, 6) QTAIL(F_Q2, 4) QTAIL(F_Q2, 2) QTAIL(F_Q2, 0)
    return;
  }

  const int q4 = (lane & 15) >> 2, p4 = lane & 3, r32 = lane & 31, hh = lane >> 5;
  const int ch = w >> 2;
  const unsigned go = lw * 1024 + lane * 16, gs = ch * 24576;
  unsigned dst[6];
#pragma unroll
  for (int i = 0; i < 6; ++i) {
    const unsigned d = 64 * (i % 3) + 32 * (i / 3) + (r32 & 19) + ((r32 & 4) << 1) + ((r32 & 8) >> 1), d6 = d / 6u;
    dst[i] = ch ? L_VP + (d - 6 * d6) * VT + lw * 1024 + hh * 512 + d6 * 16 : L_KP + i * 4096 + go;
  }
  asm volatile("" : "+v"(dst[0]), "+v"(dst[1]), "+v"(dst[2]), "+v"(dst[3]), "+v"(dst[4]), "+v"(dst[5]));
  static_assert(RING >= 4 && RING <= 6, "ring depth");
  if (ch) {
#pragma unroll 1
    for (int s = 0; s < NST - (RING - 1); ++s) {
      vmcnt<4 * (RING - 2)>();
      bars<1>();
      issue();
    }
    if (RING > 5) { vmcnt<16>(); bars<1>(); }
    if (RING > 4) { vmcnt<12>(); bars<1>(); }
    vmcnt<8>(); bars<1>();
    vmcnt<4>(); bars<1>();
    vmcnt<0>(); bars<1>();
    STAMP(3);
  } else {
    const int rq = lw & 1, cq = lw >> 1;
    const unsigned kt = (8 * hh + q4) * 128 + ((cq ^ (q4 >> 1)) << 6) + ((lane >> 4) & 1) * 32 + (p4 & 1) * 16 + (p4 >> 1) * 8;
    unsigned et[4];
#pragma unroll
    for (int kk = 0; kk < 4; ++kk) et[kk] = 8192 + (32 * rq + r32) * 128 + (((2 * kk + hh) ^ ((r32 >> 1) & 7)) << 4);
    const unsigned fin_v = tensor ? 24576 + cq * 12288 + rq * 2048 + lane * 16 : rq * 12288 + cq * 2048 + lane * 16;
    f32x16 acc, aco;
#pragma unroll
    for (int i = 0; i < 16; ++i) { acc[i] = 0.f; aco[i] = 0.f; }
    auto publish = [&](const f32x16& x, unsigned off) {
#pragma unroll
      for (int t = 0; t < 2; ++t)
        st_sc1(clean(u32x4{pk2(x[8 * t], x[8 * t + 1]), pk2(x[8 * t + 2], x[8 * t + 3]), pk2(x[8 * t + 4], x[8 * t + 5]), pk2(x[8 * t + 6], x[8 * t + 7])}),
               rsP, fin_v + off + t * 1024);
    };
    const unsigned part_c = role < 2 ? PART_OFF + role * 4096 : PAY_HEAD;
    unsigned hs_seen;
    u32x4 pa, pb;
    auto run = [&](auto ISV) {
      u32x4 ea[4], eb[4], oa[4], ob[4];
#pragma unroll
      for (int kk = 0; kk < 4; ++kk) { oa[kk] = u32x4{0u, 0u, 0u, 0u}; ob[kk] = u32x4{0u, 0u, 0u, 0u}; }
      int so = 0;
      auto mul = [&](f32x16& c, u32x4(&fa_)[4], u32x4(&fb_)[4]) {
#pragma unroll
        for (int kk = 0; kk < 4; ++kk) c = ISV.value ? mfma32(fb_[kk], fa_[kk], c) : mfma32(fa_[kk], fb_[kk], c);
      };
      auto stage = [&](u32x4(&na)[4], u32x4(&nb)[4], f32x16& c, u32x4(&fa_)[4], u32x4(&fb_)[4]) {
        bars<1>();
        LAS char* sl = L + so;
#pragma unroll
        for (int kk = 0; kk < 4; ++kk) {
          na[kk] = tr2(sl + kt + kk * 2048, sl + kt + kk * 2048 + 512);
          nb[kk] = *(LAS u32x4*)(sl + et[kk]);
        }
        __builtin_amdgcn_sched_barrier(0);
        mul(c, fa_, fb_);
        __builtin_amdgcn_sched_barrier(0);
        lgkm0();
        so += SLOT;
        if (so == RING * SLOT) so = 0;
      };
#pragma unroll 1
      for (int s = 0; s < NST - 2; s += 2) {
        stage(ea, eb, aco, oa, ob);
        if (helper && s == 16) {
          publish(acc, PART_OFF);
          publish(aco, PART_OFF + 4096);
#pragma unroll
          for (int i = 0; i < 16; ++i) { acc[i] = 0.f; aco[i] = 0.f; }
        }
        stage(oa, ob, acc, ea, eb);
      }
      hs_seen = __hip_atomic_load(flag + F_HS, RLX_WG);
      pa = ld_sc1(rsP, fin_v, part_c);
      pb = ld_sc1(rsP, fin_v + 1024, part_c);
      stage(ea, eb, aco, oa, ob);
      stage(oa, ob, acc, ea, eb);
      acc += aco;
      mul(acc, oa, ob);
    };
    if (tensor) run(BoolC<true>{});
    else run(BoolC<false>{});
    STAMP(3);
    if (__builtin_amdgcn_readfirstlane(hs_seen) == 0u) {
      lds_wait(flag + F_HS, 1u);
      pa[0] = POISON;
    }
#pragma unroll 1
    for (unsigned spins = 0; spins < SPIN_CAP; ++spins) {
      if (__builtin_amdgcn_ballot_w64(umax2(umax4(pa), umax4(pb)) == POISON) == 0ull) break;
      __builtin_amdgcn_s_sleep(8);
      asm volatile("" ::: "memory");
      pa = ld_sc1(rsP, fin_v, part_c);
      pb = ld_sc1(rsP, fin_v + 1024, part_c);
    }
#pragma unroll
    for (int i = 0; i < 4; ++i) {
      acc[2 * i] += __uint_as_float(pa[i] << 16);
      acc[2 * i + 1] += __uint_as_float(pa[i] & 0xffff0000u);
      acc[8 + 2 * i] += __uint_as_float(pb[i] << 16);
      acc[9 + 2 * i] += __uint_as_float(pb[i] & 0xffff0000u);
    }
    publish(acc, helper ? PART_OFF + 8192 : role * 4096);
    asm volatile("" ::: "memory");
  }
  bar();
  STAMP(2);
  {
    lds_wait(flag + F_GO + ch, 1u);
    if (GDELAY) __builtin_amdgcn_s_sleep(GDELAY);
    STAMP(11);
    if (ISSEARLY && lane == 0) __hip_atomic_fetch_add(flag + F_ISS, 1u, RLX_WG);
    unsigned pend = 7u;
#pragma unroll 1
    for (unsigned spins = 0; spins < SPIN_CAP; ++spins) {
      asm volatile("" ::: "memory");
      u32x4 gv[8];
#pragma unroll
      for (int i = 0; i < 6; ++i) gv[i] = ld_sc1(rsP, go, (pend >> (i % 3)) & 1u ? gs + i * 4096 : PAY_HEAD);
      gv[6] = ld_sc1(rsP, go, pend & 4u ? gs + PART_OFF + 2 * 4096 : PAY_HEAD);
      gv[7] = ld_sc1(rsP, go, pend & 4u ? gs + PART_OFF + 5 * 4096 : PAY_HEAD);
      if (!ISSEARLY && spins == 0 && lane == 0) __hip_atomic_fetch_add(flag + F_ISS, 1u, RLX_WG);
      asm volatile("" : "+v"(gv[0]), "+v"(gv[1]), "+v"(gv[2]), "+v"(gv[3]), "+v"(gv[4]), "+v"(gv[5]), "+v"(gv[6]), "+v"(gv[7]));
#pragma unroll
      for (int c = 0; c < 2; ++c) {
        if (((pend >> c) & 1u) && __builtin_amdgcn_ballot_w64(umax2(umax4(gv[c]), umax4(gv[3 + c])) == POISON) == 0ull) {
          *(LAS u32x4*)(L + dst[c]) = gv[c];
          *(LAS u32x4*)(L + dst[3 + c]) = gv[3 + c];
          pend &= ~(1u << c);
        }
      }
      if ((pend & 4u) && __builtin_amdgcn_ballot_w64(umax2(umax2(umax4(gv[2]), umax4(gv[5])), umax2(umax4(gv[6]), umax4(gv[7]))) == POISON) == 0ull) {
        *(LAS u32x4*)(L + dst[2]) =
            u32x4{addbf2(gv[2][0], gv[6][0]), addbf2(gv[2][1], gv[6][1]), addbf2(gv[2][2], gv[6][2]), addbf2(gv[2][3], gv[6][3])};
        *(LAS u32x4*)(L + dst[5]) =
            u32x4{addbf2(gv[5][0], gv[7][0]), addbf2(gv[5][1], gv[7][1]), addbf2(gv[5][2], gv[7][2]), addbf2(gv[5][3], gv[7][3])};
        pend &= 3u;
      }
      if (pend == 0u) break;
      if (RETRY_SLEEP) __builtin_amdgcn_s_sleep(RETRY_SLEEP);
    }
    lgkm0();
    if (lane == 0) __hip_atomic_fetch_add(flag + F_KP + ch, 1u, RLX_WG);
  }
  STAMP(4);
  LAS char* scr = L + (w < 4 ? L_SCR_LO : L_SCR_HI) + lw * 12288;
  unsigned fa[4];
#pragma unroll
  for (int k4 = 0; k4 < 4; ++k4) fa[k4] = r32 * 384 + (((2 * k4 + hh) ^ ((r32 >> 1) & 7)) << 4);
  lds_wait(flag + (ch ? F_Q2 : F_Q), 2 * lw + 2);
  STAMP(5);
  u32x4 qf[12];
#pragma unroll
  for (int ks = 0; ks < 12; ++ks) qf[ks] = *(LAS u32x4*)(scr + fa[ks & 3] + (ks >> 2) * 128);
  lds_wait(flag + F_KP, 4u);
  STAMP(6);

  f32x16 s0, s1;
#pragma unroll
  for (int i = 0; i < 16; ++i) { s0[i] = 0.f; s1[i] = 0.f; }
  {
    LAS u32x4* kf = (LAS u32x4*)(L + L_KP) + lane;
    u32x4 ka[24];
#pragma unroll
    for (int i = 0; i < 6; ++i) ka[i] = kf[i * 64];
    __builtin_amdgcn_sched_barrier(0);
#pragma unroll
    for (int i = 0; i < 24; ++i) {
      if (i + 6 < 24) ka[i + 6] = kf[(i + 6) * 64];
      if (i < 12) s0 = mfma32(ka[i], qf[i], s0);
      else s1 = mfma32(ka[i], qf[i - 12], s1);
      __builtin_amdgcn_sched_barrier(0);
    }
  }
  float m = s0[0];
#pragma unroll
  for (int i = 1; i < 16; ++i) m = fmaxf(m, s0[i]);
#pragma unroll
  for (int i = 0; i < 16; ++i) m = fmaxf(m, s1[i]);
  {
    auto rr = __builtin_amdgcn_permlane32_swap(__float_as_uint(m), __float_as_uint(m), false, false);
    m = fmaxf(__uint_as_float(rr[0]), __uint_as_float(rr[1]));
  }
  const float cs = 0.07216878364870322f * 1.4426950408889634f;
  const float mc = m * cs;
  float sum = 0.f;
#pragma unroll
  for (int i = 0; i < 16; ++i) { s0[i] = __builtin_amdgcn_exp2f(__builtin_fmaf(s0[i], cs, -mc)); sum += s0[i]; }
#pragma unroll
  for (int i = 0; i < 16; ++i) { s1[i] = __builtin_amdgcn_exp2f(__builtin_fmaf(s1[i], cs, -mc)); sum += s1[i]; }
  {
    auto rr = __builtin_amdgcn_permlane32_swap(__float_as_uint(sum), __float_as_uint(sum), false, false);
    sum = __uint_as_float(rr[0]) + __uint_as_float(rr[1]);
  }
  const float inv = 1.0f / sum;
  u32x4 pf[4];
#pragma unroll
  for (int t = 0; t < 2; ++t) {
    pf[t] = u32x4{pk2(s0[8 * t] * inv, s0[8 * t + 1] * inv), pk2(s0[8 * t + 2] * inv, s0[8 * t + 3] * inv),
                  pk2(s0[8 * t + 4] * inv, s0[8 * t + 5] * inv), pk2(s0[8 * t + 6] * inv, s0[8 * t + 7] * inv)};
    pf[2 + t] = u32x4{pk2(s1[8 * t] * inv, s1[8 * t + 1] * inv), pk2(s1[8 * t + 2] * inv, s1[8 * t + 3] * inv),
                      pk2(s1[8 * t + 4] * inv, s1[8 * t + 5] * inv), pk2(s1[8 * t + 6] * inv, s1[8 * t + 7] * inv)};
  }
  STAMP(7);
  lds_wait(flag + F_VP, 4u);
  STAMP(8);
  f32x16 o[6];
#pragma unroll
  for (int t = 0; t < 6; ++t)
#pragma unroll
    for (int i = 0; i < 16; ++i) o[t][i] = 0.f;
  {
    LAS char* vb = L + L_VP + lane * 16;
    u32x4 va[24];
#pragma unroll
    for (int i = 0; i < 6; ++i) va[i] = *(LAS u32x4*)(vb + (i % 6) * VT + (i / 6) * 1024);
    __builtin_amdgcn_sched_barrier(0);
#pragma unroll
    for (int i = 0; i < 24; ++i) {
      if (i + 6 < 24) va[i + 6] = *(LAS u32x4*)(vb + ((i + 6) % 6) * VT + ((i + 6) / 6) * 1024);
      o[i % 6] = mfma32(pf[i / 6], va[i], o[i % 6]);
      __builtin_amdgcn_sched_barrier(0);
    }
  }
  const __amdgpu_buffer_rsrc_t rsO =
      __builtin_amdgcn_make_buffer_rsrc(out + ((size_t)head * SQ + sub * 256 + w * 32) * ROWB, 0, 32 * ROWB, 0x00020000);
  const unsigned ov = hh * 4 * ROWB + r32 * 12;
#pragma unroll
  for (int j = 0; j < 16; ++j) {
    const u32x3 x = {pk2(o[0][j], o[1][j]), pk2(o[2][j], o[3][j]), pk2(o[4][j], o[5][j])};
    __builtin_amdgcn_raw_buffer_store_b96(x, rsO, ov + ((j & 3) + 8 * (j >> 2)) * ROWB, 0, 2);
  }
}

extern "C" void kernel_launch(void* const* d_in, const int* in_sizes, int n_in, void* d_out, int out_size, void* d_ws, size_t ws_size,
                              hipStream_t stream) {
  lin_kernel<<<256, 704, 0, stream>>>((const char*)d_in[0], (const char*)d_in[1], (const char*)d_in[2], (const char*)d_in[3],
                                      (const char*)d_in[4], (char*)d_ws, (char*)d_out);
}
```

```cpp
#include <hip/hip_runtime.h>
#include <hip/hip_bf16.h>

typedef short s16x4 __attribute__((ext_vector_type(4)));
typedef __bf16 bf16x8 __attribute__((ext_vector_type(8)));
typedef __bf16 bf16x2 __attribute__((ext_vector_type(2)));
typedef float f32x2 __attribute__((ext_vector_type(2)));
typedef float f32x4 __attribute__((ext_vector_type(4)));
typedef float f32x16 __attribute__((ext_vector_type(16)));
typedef unsigned u32x4 __attribute__((ext_vector_type(4)));
typedef unsigned u32x2 __attribute__((ext_vector_type(2)));
typedef unsigned u32x3 __attribute__((ext_vector_type(3)));
#define LAS __attribute__((address_space(3)))
#define GAS __attribute__((address_space(1)))
#define DI __device__ __forceinline__
#define RLX_AGENT __ATOMIC_RELAXED, __HIP_MEMORY_SCOPE_AGENT
#define RLX_WG __ATOMIC_RELAXED, __HIP_MEMORY_SCOPE_WORKGROUP
typedef GAS unsigned gu32;

#define RING 6
#define QPRE 1
#define POLLGAP 3
#define RETRY_SLEEP 32
#define W1 24
#define S2START 6
#define QSTART 4
#define W2 12
#define LATEPUB 20
#define QFEARLY 1
#define SMX 1
#define ISSAT 0
#define VDELAY 0
#define STAMPS 0

constexpr int H = 32, SQ = 2048, SKV = 2048, DH = 192, R = 64;
constexpr int ROWB = DH * 2;
constexpr int SLOT = 16384;
constexpr int NST = 24;
constexpr int PART_OFF = 49152;
constexpr int PAY_HEAD = 2 * 49152;
constexpr int CTL_OFF = H * PAY_HEAD;
constexpr int CTL_HEAD = 17 * 128;
constexpr int DBG_OFF = CTL_OFF + H * CTL_HEAD + 4096;
constexpr unsigned POISON = 0xFFFFFFFFu;
constexpr unsigned SPIN_CAP = 1u << 20;
constexpr int VT = 4096 + 16;
constexpr int L_KP = 0, L_VP = 24576, L_SCR_HI = 49152 + 256, L_SCR_LO = L_SCR_HI + 49152, L_FLAG = L_SCR_LO + 49152, L_BYTES = L_FLAG + 64;
constexpr int F_ISS = 0, F_KP = 1, F_VP = 2, F_Q = 3, F_GO = 4  , F_Q2 = 6  ;

template <bool B> struct BoolC { static constexpr bool value = B; };
DI unsigned pk2(float a, float b) {
  f32x2 f = {a, b};
  return __builtin_bit_cast(unsigned, __builtin_convertvector(f, bf16x2));
}
DI f32x4 mfma16(u32x4 a, u32x4 b, f32x4 c) {
  return __builtin_amdgcn_mfma_f32_16x16x32_bf16(__builtin_bit_cast(bf16x8, a), __builtin_bit_cast(bf16x8, b), c, 0, 0, 0);
}
DI f32x16 mfma32(u32x4 a, u32x4 b, f32x16 c) {
  return __builtin_amdgcn_mfma_f32_32x32x16_bf16(__builtin_bit_cast(bf16x8, a), __builtin_bit_cast(bf16x8, b), c, 0, 0, 0);
}
DI u32x4 tr2(LAS char* p0, LAS char* p1) {
  u32x2 l2 = __builtin_bit_cast(u32x2, __builtin_amdgcn_ds_read_tr16_b64_v4i16((LAS s16x4*)p0));
  u32x2 h2 = __builtin_bit_cast(u32x2, __builtin_amdgcn_ds_read_tr16_b64_v4i16((LAS s16x4*)p1));
  return u32x4{l2[0], l2[1], h2[0], h2[1]};
}
DI void dma_nt(unsigned lds, unsigned voff, __amdgpu_buffer_rsrc_t rs, unsigned soff) {
  unsigned keep;
  asm volatile("s_mov_b32 %0, m0\n\ts_mov_b32 m0, %1\n\ts_nop 0\n\tbuffer_load_dwordx4 %2, %3, %4 offen nt lds\n\ts_mov_b32 m0, %0"
               : "=&s"(keep) : "s"(lds), "v"(voff), "s"(rs), "s"(soff) : "memory");
}
DI void dma(unsigned lds, unsigned voff, __amdgpu_buffer_rsrc_t rs, unsigned soff) {
  unsigned keep;
  asm volatile("s_mov_b32 %0, m0\n\ts_mov_b32 m0, %1\n\ts_nop 0\n\tbuffer_load_dwordx4 %2, %3, %4 offen lds\n\ts_mov_b32 m0, %0"
               : "=&s"(keep) : "s"(lds), "v"(voff), "s"(rs), "s"(soff) : "memory");
}
template <int N> DI void vmcnt() { asm volatile("s_waitcnt vmcnt(%0)" ::"n"(N) : "memory"); }
DI void lgkm0() { asm volatile("s_waitcnt lgkmcnt(0)" ::: "memory"); }
DI void bar() {
  lgkm0();
  __builtin_amdgcn_s_barrier();
  asm volatile("" ::: "memory");
}
template <int N> DI void bars() {
#pragma unroll
  for (int i = 0; i < N; ++i) __builtin_amdgcn_s_barrier();
  asm volatile("" ::: "memory");
}
DI u32x4 ld_sc1(__amdgpu_buffer_rsrc_t rs, unsigned voff, unsigned soff) { return __builtin_amdgcn_raw_buffer_load_b128(rs, voff, soff, 16); }
DI void st_sc1(u32x4 v, __amdgpu_buffer_rsrc_t rs, unsigned voff) { __builtin_amdgcn_raw_buffer_store_b128(v, rs, voff, 0, 16); }
DI unsigned addbf2(unsigned x, unsigned y) {
  const float lo = __uint_as_float(x << 16) + __uint_as_float(y << 16);
  const float hi = __uint_as_float(x & 0xffff0000u) + __uint_as_float(y & 0xffff0000u);
  return pk2(lo, hi);
}
DI unsigned umin2(unsigned a, unsigned b) { return a < b ? a : b; }
DI unsigned umax2(unsigned a, unsigned b) { return a > b ? a : b; }
DI unsigned umax4(u32x4 v) { return umax2(umax2(v[0], v[1]), umax2(v[2], v[3])); }
DI bool has_poison(u32x4 v) { return __builtin_amdgcn_ballot_w64(umax4(v) == POISON) != 0ull; }
DI u32x4 clean(u32x4 v) { return u32x4{umin2(v[0], POISON - 1u), umin2(v[1], POISON - 1u), umin2(v[2], POISON - 1u), umin2(v[3], POISON - 1u)}; }
DI unsigned at_read(gu32* p) {
  unsigned z = 0u;
  asm volatile("" : "+v"(z));
  return __hip_atomic_fetch_add(p, z, RLX_AGENT);
}
DI void at_write(gu32* p, unsigned v) { asm volatile("global_atomic_swap %0, %1, off" ::"v"(p), "v"(v) : "memory"); }
DI void lds_wait(LAS unsigned* p, unsigned want) {
#pragma unroll 1
  for (unsigned spins = 0; spins < (SPIN_CAP << 6); ++spins) {
    if ((unsigned)__builtin_amdgcn_readfirstlane(__hip_atomic_load(p, RLX_WG)) >= want) break;
    __builtin_amdgcn_s_sleep(1);
  }
  asm volatile("" ::: "memory");
}
#define STAMP(i) do { } while (0)

__global__ void __launch_bounds__(768, 3) lin_kernel(const char* __restrict__ q, const char* __restrict__ k, const char* __restrict__ v,
                                                     const char* __restrict__ e, const char* __restrict__ f, char* ws,
                                                     char* __restrict__ out) {
  __shared__ __attribute__((aligned(1024))) char smem[L_BYTES];
  LAS char* L = (LAS char*)smem;
  LAS unsigned* flag = (LAS unsigned*)(L + L_FLAG);
  STAMP(0);
  const int b = blockIdx.x, xcd = b & 7, jj = b >> 3, head = xcd * 4 + (jj >> 3), sub = jj & 7;
  const int tensor = sub >> 2, role = sub & 3;
  const int tid = threadIdx.x, lane = tid & 63, w = __builtin_amdgcn_readfirstlane(tid >> 6);
  const bool helper = role == 3;
  const int lw = w & 3;
  const unsigned ldsb = (unsigned)(size_t)L;
  const __amdgpu_buffer_rsrc_t rsK =
      __builtin_amdgcn_make_buffer_rsrc((char*)(tensor ? v : k) + (size_t)head * SKV * ROWB, 0, SKV * ROWB, 0x00020000);
  const __amdgpu_buffer_rsrc_t rsE = __builtin_amdgcn_make_buffer_rsrc((char*)(tensor ? f : e), 0, R * SKV * 2, 0x00020000);
  unsigned sk = helper ? 1536u * ROWB : role * 128u, se = helper ? 3072u : 0u;
  int pairs = helper ? 16 : 0;
  unsigned voK[2], voE[2];
#pragma unroll
  for (int x = 0; x < 2; ++x) {
    const int row = 8 * (2 * lw + x) + (lane >> 3);
    voK[x] = row * ROWB + (((lane & 7) ^ (((lane >> 4) & 1) << 2)) << 4);
    voE[x] = row * (SKV * 2) + (((lane & 7) ^ ((x << 2) | (lane >> 4))) << 4);
  }
  unsigned dsl = ldsb + lw * 2048;
  auto issue = [&]() {
    dma_nt(dsl, voK[0], rsK, sk);
    dma_nt(dsl + 1024, voK[1], rsK, sk);
    dma(dsl + 8192, voE[0], rsE, se);
    dma(dsl + 9216, voE[1], rsE, se);
    if (pairs > 0) {
      --pairs;
      sk += pairs & 1 ? 128u : pairs ? 64u * ROWB - 128u : 128u - 448u * ROWB;
      se += pairs & 1 ? 0u : pairs ? 128u : -896u;
    } else {
      sk += 64u * ROWB;
      se += 128u;
    }
    dsl += SLOT;
    if (dsl >= ldsb + RING * SLOT) dsl -= RING * SLOT;
  };
  if ((w & 12) == 4) {
#pragma unroll
    for (int s = 0; s < RING - 1; ++s) issue();
  }
  __builtin_amdgcn_sched_barrier(0);
  const __amdgpu_buffer_rsrc_t rsP = __builtin_amdgcn_make_buffer_rsrc(ws + (size_t)head * PAY_HEAD, 0, PAY_HEAD, 0x00020000);
  gu32* ctl = (gu32*)(ws + CTL_OFF + head * CTL_HEAD);

  const __amdgpu_buffer_rsrc_t rsQ =
      __builtin_amdgcn_make_buffer_rsrc((char*)q + ((size_t)head * SQ + sub * 256) * ROWB, 0, 256 * ROWB, 0x00020000);
  unsigned voQ[6];
  auto qprep = [&]() {
#pragma unroll
    for (int bb = 0; bb < 3; ++bb) {
      const int idx = 64 * bb + lane, row = idx / 24, c = idx - row * 24;
      voQ[bb] = row * 384 + (((c & 24) | ((c & 7) ^ (row >> 1))) << 4);
      voQ[3 + bb] = (voQ[bb] ^ 64u) + 3072u;
    }
    asm volatile("" : "+v"(voQ[0]), "+v"(voQ[1]), "+v"(voQ[2]), "+v"(voQ[3]), "+v"(voQ[4]), "+v"(voQ[5]));
  };
  auto qgroup = [&](int j) {
    const unsigned d = ldsb + (j < 8 ? L_SCR_LO : L_SCR_HI - 49152) + j * 6144;
#pragma unroll
    for (int i = 0; i < 6; ++i) dma_nt(d + i * 1024, voQ[i], rsQ, j * 6144);
  };

  if (__builtin_expect(w == 8, 0)) {
    const int l8 = lane & 7;
    gu32* nonce_p = ctl + l8;
    gu32* inbox_p = ctl + (1 + 2 * sub + (l8 >> 2)) * 32 + (l8 & 3);
    gu32* note_p = ctl + (1 + 2 * l8 + tensor) * 32 + role;
    const unsigned old = __hip_atomic_load(lane < 8 ? inbox_p : ctl + sub, RLX_AGENT);
    if (lane < 8) flag[lane] = 0u;
    const u32x4 pz = {POISON, POISON, POISON, POISON};
#pragma unroll 1
    for (int s = helper ? 0 : 2; s < 3; ++s) {
      const unsigned base = lane * 16 + tensor * 24576 + (helper ? PART_OFF + s * 4096 : role * 4096);
#pragma unroll
      for (int i = 0; i < 8; ++i) st_sc1(pz, rsP, base + (i >> 2) * 12288 + (i & 3) * 1024);
    }
    lgkm0();
    bars<1>();
    unsigned n = __builtin_amdgcn_readlane(old, 8) + 1u;
    while (__builtin_amdgcn_ballot_w64(old == n) != 0ull) ++n;
    if (lane == 0) at_write(ctl + sub, n);
    bars<9>();
    unsigned echoed = __hip_atomic_load(nonce_p, RLX_AGENT);
    bars<6>();
    vmcnt<0>();
    asm volatile("" : "+v"(echoed));
    bars<8>();
    if (lane < 8) at_write(note_p, echoed);
    auto refresh = [&]() {
      const unsigned cur = __hip_atomic_load(nonce_p, RLX_AGENT);
      if (lane < 8 && cur != echoed) at_write(note_p, cur);
      echoed = cur;
    };
    bars<1>();
    unsigned seen = 0u;
#pragma unroll 1
    for (unsigned spins = 0; spins < (SPIN_CAP >> 3); ++spins) {
      if (lane < 8) {
        unsigned c0, c1, c2;
#define POLL_STEP(NEW, OLD)                                                                                                        \
  "global_atomic_add " NEW ", %4, %5, off sc0\n\ts_sleep %6\n\ts_waitcnt vmcnt(2)\n\tv_cmp_eq_u32 vcc, %9, " OLD "\n\t"           \
  "s_or_b32 %3, %3, vcc_lo\n\tv_and_b32 " OLD ", %3, %10\n\tv_cmp_eq_u32 vcc, " OLD ", %10\n\t"                                  \
  "s_and_saveexec_b64 s[6:7], vcc\n\tds_write_b32 %7, %8\n\ts_mov_b64 exec, s[6:7]\n\ts_cmp_eq_u32 %3, 0xff\n\ts_cbranch_scc1 2f\n\t"
        asm volatile(
            "global_atomic_add %0, %4, %5, off sc0\n\ts_sleep %6\n\t"
            "global_atomic_add %1, %4, %5, off sc0\n\ts_sleep %6\n\t"
            "s_mov_b32 s4, 7\n"
            "1:\n\t" POLL_STEP("%2", "%0") POLL_STEP("%0", "%1") POLL_STEP("%1", "%2")
            "s_sub_u32 s4, s4, 1\n\ts_cbranch_scc0 1b\n"
            "2:\n\t"
            "s_waitcnt vmcnt(0) lgkmcnt(0)"
            : "=&v"(c0), "=&v"(c1), "=&v"(c2), "+s"(seen)
            : "v"(inbox_p), "v"(0u), "n"(POLLGAP), "v"(ldsb + L_FLAG + (F_GO + (l8 >> 2)) * 4), "v"(1u), "s"(n), "v"(l8 & 4 ? 0xf0u : 0x0fu)
            : "memory", "vcc", "scc", "s4", "s6", "s7");
      }
      seen = __builtin_amdgcn_readfirstlane(seen);
      refresh();
      if (seen == 0xffu) break;
    }
    if (lane < 2) __hip_atomic_store(flag + F_GO + lane, 1u, RLX_WG);
    return;
  }
  if (__builtin_expect(w == 9, 0)) {
    qprep();
    bars<NST + 1 - QSTART>();
    int j = 0;
#pragma unroll 1
    for (int i = 0; i < QSTART; ++i) {
#pragma unroll 1
      for (int x = 0; x < QPRE && j < 8; ++x, ++j) qgroup(j);
      bars<1>();
    }
#pragma unroll 1
    for (; j < 8; ++j) {
      if ((unsigned)__builtin_amdgcn_readfirstlane(__hip_atomic_load(flag + F_ISS, RLX_WG)) < 8u) {
        vmcnt<W1 - 6>();
        if (j + 1 - W1 / 6 > 0 && lane == 0) __hip_atomic_store(flag + F_Q, (unsigned)(j + 1 - W1 / 6), RLX_WG);
      }
      qgroup(j);
    }
#define QTAIL(F, N)                                                        \
  vmcnt<6 * (N)>();                                                        \
  if (lane == 0) __hip_atomic_store(flag + (F), 8u - (N), RLX_WG);
    vmcnt<36>();
    if (lane == 0) __hip_atomic_fetch_max(flag + F_Q, 2u, RLX_WG);
    vmcnt<24>();
    if (lane == 0) __hip_atomic_fetch_max(flag + F_Q, 4u, RLX_WG);
    QTAIL(F_Q, 3) QTAIL(F_Q, 2) QTAIL(F_Q, 1) QTAIL(F_Q, 0)
    return;
  }
  if (__builtin_expect(w >= 10, 0)) {
    qprep();
    bars<NST + 1>();
    bool wide = false;
#pragma unroll 1
    for (unsigned spins = 0; spins < (SPIN_CAP << 6); ++spins) {
      wide = (unsigned)__builtin_amdgcn_readfirstlane(__hip_atomic_load(flag + F_ISS, RLX_WG)) >= 8u;
      if (wide || (unsigned)__builtin_amdgcn_readfirstlane(__hip_atomic_load(flag + F_Q, RLX_WG)) >= S2START) break;
      __builtin_amdgcn_s_sleep(1);
    }
#pragma unroll 1
    for (int x = 0; x < 4; ++x) {
      if (!wide) {
        vmcnt<W2 - 6>();
        wide = (unsigned)__builtin_amdgcn_readfirstlane(__hip_atomic_load(flag + F_ISS, RLX_WG)) >= 8u;
      }
      qgroup(8 + 2 * (w - 10) + (x & 1) + 4 * (x >> 1));
    }
    vmcnt<12>();
    if (lane == 0) __hip_atomic_store(flag + F_Q2 + (w - 10), 1u, RLX_WG);
    vmcnt<0>();
    if (lane == 0) __hip_atomic_store(flag + F_Q2 + (w - 10), 2u, RLX_WG);
    return;
  }

  const int q4 = (lane & 15) >> 2, p4 = lane & 3, r32 = lane & 31, hh = lane >> 5;
  const int ch = w >> 2;
  const unsigned go = lw * 1024 + lane * 16, gs = ch * 24576;
  unsigned dst[6];
#pragma unroll
  for (int i = 0; i < 6; ++i) {
    const unsigned d = 64 * (i % 3) + 32 * (i / 3) + (r32 & 19) + ((r32 & 4) << 1) + ((r32 & 8) >> 1), d6 = d / 6u;
    dst[i] = ch ? L_VP + (d - 6 * d6) * VT + lw * 1024 + hh * 512 + d6 * 16 : L_KP + i * 4096 + go;
  }
  asm volatile("" : "+v"(dst[0]), "+v"(dst[1]), "+v"(dst[2]), "+v"(dst[3]), "+v"(dst[4]), "+v"(dst[5]));
  static_assert(RING >= 4 && RING <= 6, "ring depth");
  if (ch) {
#pragma unroll 1
    for (int s = 0; s < NST - (RING - 1); ++s) {
      vmcnt<4 * (RING - 2)>();
      bars<1>();
      issue();
    }
    if (RING > 5) { vmcnt<16>(); bars<1>(); }
    if (RING > 4) { vmcnt<12>(); bars<1>(); }
    vmcnt<8>(); bars<1>();
    vmcnt<4>(); bars<1>();
    vmcnt<0>(); bars<1>();
    STAMP(3);
  } else {
    const int rq = lw & 1, cq = lw >> 1;
    const unsigned kt = (8 * hh + q4) * 128 + ((cq ^ (q4 >> 1)) << 6) + ((lane >> 4) & 1) * 32 + (p4 & 1) * 16 + (p4 >> 1) * 8;
    unsigned et[4];
#pragma unroll
    for (int kk = 0; kk < 4; ++kk) et[kk] = 8192 + (32 * rq + r32) * 128 + (((2 * kk + hh) ^ ((r32 >> 1) & 7)) << 4);
    const unsigned fin_v = tensor ? 24576 + cq * 12288 + rq * 2048 + lane * 16 : rq * 12288 + cq * 2048 + lane * 16;
    f32x16 acc, aco;
#pragma unroll
    for (int i = 0; i < 16; ++i) { acc[i] = 0.f; aco[i] = 0.f; }
    auto pack = [&](const f32x16& x, int t) {
      return clean(u32x4{pk2(x[8 * t], x[8 * t + 1]), pk2(x[8 * t + 2], x[8 * t + 3]), pk2(x[8 * t + 4], x[8 * t + 5]), pk2(x[8 * t + 6], x[8 * t + 7])});
    };
    auto run = [&](auto ISV) {
      u32x4 ea[4], eb[4], oa[4], ob[4];
#pragma unroll
      for (int kk = 0; kk < 4; ++kk) { oa[kk] = u32x4{0u, 0u, 0u, 0u}; ob[kk] = u32x4{0u, 0u, 0u, 0u}; }
      int so = 0;
      auto mul = [&](f32x16& c, u32x4(&fa_)[4], u32x4(&fb_)[4]) {
#pragma unroll
        for (int kk = 0; kk < 4; ++kk) c = ISV.value ? mfma32(fb_[kk], fa_[kk], c) : mfma32(fa_[kk], fb_[kk], c);
      };
      auto stage = [&](u32x4(&na)[4], u32x4(&nb)[4], f32x16& c, u32x4(&fa_)[4], u32x4(&fb_)[4]) {
        bars<1>();
        LAS char* sl = L + so;
#pragma unroll
        for (int kk = 0; kk < 4; ++kk) {
          na[kk] = tr2(sl + kt + kk * 2048, sl + kt + kk * 2048 + 512);
          nb[kk] = *(LAS u32x4*)(sl + et[kk]);
        }
        __builtin_amdgcn_sched_barrier(0);
        mul(c, fa_, fb_);
        __builtin_amdgcn_sched_barrier(0);
        lgkm0();
        so += SLOT;
        if (so == RING * SLOT) so = 0;
      };
      u32x4 held[4];
#pragma unroll
      for (int i = 0; i < 4; ++i) held[i] = u32x4{0u, 0u, 0u, 0u};
#pragma unroll 1
      for (int s = 0; s < NST; s += 2) {
        stage(ea, eb, aco, oa, ob);
        if (helper && s == 16) {
          held[0] = pack(acc, 0); held[1] = pack(acc, 1); held[2] = pack(aco, 0); held[3] = pack(aco, 1);
#pragma unroll
          for (int i = 0; i < 16; ++i) { acc[i] = 0.f; aco[i] = 0.f; }
        }
        if (helper && s == LATEPUB) {
#pragma unroll
          for (int i = 0; i < 4; ++i) st_sc1(held[i], rsP, fin_v + PART_OFF + (i >> 1) * 4096 + (i & 1) * 1024);
        }
        stage(oa, ob, acc, ea, eb);
      }
      mul(aco, oa, ob);
    };
    if (tensor) run(BoolC<true>{});
    else run(BoolC<false>{});
    STAMP(3);
    acc += aco;
    const unsigned fin_o = fin_v + (helper ? PART_OFF + 8192 : role * 4096);
    st_sc1(pack(acc, 0), rsP, fin_o);
    st_sc1(pack(acc, 1), rsP, fin_o + 1024);
    asm volatile("" ::: "memory");
  }
  bar();
  STAMP(2);
  LAS char* scr = L + (w < 4 ? L_SCR_LO : L_SCR_HI) + lw * 12288;
  unsigned fa[4];
#pragma unroll
  for (int k4 = 0; k4 < 4; ++k4) fa[k4] = r32 * 384 + (((2 * k4 + hh) ^ ((r32 >> 1) & 7)) << 4);
  LAS unsigned* qflag = flag + (ch ? F_Q2 + (lw & 1) : F_Q);
  const unsigned qwant = ch ? (lw >> 1) + 1 : 2 * lw + 2;
  lds_wait(flag + F_GO + ch, 1u);
  if (VDELAY && ch) __builtin_amdgcn_s_sleep(VDELAY);
  STAMP(11);
  if (ISSAT == 0 && lane == 0) __hip_atomic_fetch_add(flag + F_ISS, 1u, RLX_WG);
#pragma unroll 1
  for (unsigned spins = 0; spins < SPIN_CAP; ++spins) {
    asm volatile("" ::: "memory");
    u32x4 gv[12];
#pragma unroll
    for (int i = 0; i < 12; ++i) gv[i] = ld_sc1(rsP, go, gs + (i >> 1) * 4096 + (i & 1) * PART_OFF);
    if (ISSAT == 1 && spins == 0 && lane == 0) __hip_atomic_fetch_add(flag + F_ISS, 1u, RLX_WG);
    __builtin_amdgcn_sched_barrier(0);
    unsigned mx = 0u;
#pragma unroll
    for (int i = 0; i < 6; ++i) {
      const u32x4 x = gv[2 * i], y = gv[2 * i + 1];
      mx = umax2(mx, umax2(umax4(x), umax4(y)));
      *(LAS u32x4*)(L + dst[i]) = u32x4{addbf2(x[0], y[0]), addbf2(x[1], y[1]), addbf2(x[2], y[2]), addbf2(x[3], y[3])};
      __builtin_amdgcn_sched_barrier(0);
    }
    if (__builtin_amdgcn_ballot_w64(mx == POISON) == 0ull) break;
    __builtin_amdgcn_s_sleep(RETRY_SLEEP);
  }
  lgkm0();
  if (lane == 0) __hip_atomic_fetch_add(flag + F_KP + ch, 1u, RLX_WG);
  STAMP(4);
  lds_wait(qflag, qwant);
  STAMP(5);
  u32x4 qf[12];
#pragma unroll
  for (int ks = 0; ks < 12; ++ks) qf[ks] = *(LAS u32x4*)(scr + fa[ks & 3] + (ks >> 2) * 128);
  lds_wait(flag + F_KP, 4u);
  STAMP(6);

  const float cs = 0.07216878364870322f * 1.4426950408889634f;
  auto xmax = [](float x) {
    auto rr = __builtin_amdgcn_permlane32_swap(__float_as_uint(x), __float_as_uint(x), false, false);
    return fmaxf(__uint_as_float(rr[0]), __uint_as_float(rr[1]));
  };
  f32x16 s0, s1;
#pragma unroll
  for (int i = 0; i < 16; ++i) { s0[i] = 0.f; s1[i] = 0.f; }
  float m0, sum0 = 0.f;
  {
    LAS u32x4* kf = (LAS u32x4*)(L + L_KP) + lane;
    u32x4 ka[24];
#pragma unroll
    for (int i = 0; i < 6; ++i) ka[i] = kf[i * 64];
    __builtin_amdgcn_sched_barrier(0);
#pragma unroll
    for (int i = 0; i < 12; ++i) {
      ka[i + 6] = kf[(i + 6) * 64];
      s0 = mfma32(ka[i], qf[i], s0);
      __builtin_amdgcn_sched_barrier(0);
    }
#pragma unroll
    for (int i = 12; i < 24; ++i) {
      if (i + 6 < 24) ka[i + 6] = kf[(i + 6) * 64];
      s1 = mfma32(ka[i], qf[i - 12], s1);
      if (i == 13) {
        m0 = s0[0];
#pragma unroll
        for (int j = 1; j < 8; ++j) m0 = fmaxf(m0, s0[j]);
      }
      if (i == 14) {
#pragma unroll
        for (int j = 8; j < 16; ++j) m0 = fmaxf(m0, s0[j]);
      }
      if (i == 15) m0 = xmax(m0) * cs;
      if (i >= 16) {
#pragma unroll
        for (int j = 2 * (i - 16); j < 2 * (i - 16) + 2; ++j) { s0[j] = __builtin_amdgcn_exp2f(__builtin_fmaf(s0[j], cs, -m0)); sum0 += s0[j]; }
      }
      __builtin_amdgcn_sched_barrier(0);
    }
  }
  float m = s1[0];
#pragma unroll
  for (int i = 1; i < 16; ++i) m = fmaxf(m, s1[i]);
  const float mc = fmaxf(xmax(m) * cs, m0);
  const float a0 = __builtin_amdgcn_exp2f(m0 - mc);
  float sum = sum0 * a0;
#pragma unroll
  for (int i = 0; i < 16; ++i) { s1[i] = __builtin_amdgcn_exp2f(__builtin_fmaf(s1[i], cs, -mc)); sum += s1[i]; }
  {
    auto rr = __builtin_amdgcn_permlane32_swap(__float_as_uint(sum), __float_as_uint(sum), false, false);
    sum = __uint_as_float(rr[0]) + __uint_as_float(rr[1]);
  }
  const float inv = __builtin_amdgcn_rcpf(sum), inv0 = inv * a0;
  u32x4 pf[4];
#pragma unroll
  for (int t = 0; t < 2; ++t) {
    pf[t] = u32x4{pk2(s0[8 * t] * inv0, s0[8 * t + 1] * inv0), pk2(s0[8 * t + 2] * inv0, s0[8 * t + 3] * inv0),
                  pk2(s0[8 * t + 4] * inv0, s0[8 * t + 5] * inv0), pk2(s0[8 * t + 6] * inv0, s0[8 * t + 7] * inv0)};
    pf[2 + t] = u32x4{pk2(s1[8 * t] * inv, s1[8 * t + 1] * inv), pk2(s1[8 * t + 2] * inv, s1[8 * t + 3] * inv),
                      pk2(s1[8 * t + 4] * inv, s1[8 * t + 5] * inv), pk2(s1[8 * t + 6] * inv, s1[8 * t + 7] * inv)};
  }
  STAMP(7);
  lds_wait(flag + F_VP, 4u);
  STAMP(8);
  f32x16 o[6];
#pragma unroll
  for (int t = 0; t < 6; ++t)
#pragma unroll
    for (int i = 0; i < 16; ++i) o[t][i] = 0.f;
  {
    LAS char* vb = L + L_VP + lane * 16;
    u32x4 va[24];
#pragma unroll
    for (int i = 0; i < 6; ++i) va[i] = *(LAS u32x4*)(vb + (i % 6) * VT + (i / 6) * 1024);
    __builtin_amdgcn_sched_barrier(0);
#pragma unroll
    for (int i = 0; i < 24; ++i) {
      if (i + 6 < 24) va[i + 6] = *(LAS u32x4*)(vb + ((i + 6) % 6) * VT + ((i + 6) / 6) * 1024);
      o[i % 6] = mfma32(pf[i / 6], va[i], o[i % 6]);
      __builtin_amdgcn_sched_barrier(0);
    }
  }
  const __amdgpu_buffer_rsrc_t rsO =
      __builtin_amdgcn_make_buffer_rsrc(out + ((size_t)head * SQ + sub * 256 + w * 32) * ROWB, 0, 32 * ROWB, 0x00020000);
  const unsigned ov = hh * 4 * ROWB + r32 * 12;
#pragma unroll
  for (int j = 0; j < 16; ++j) {
    const u32x3 x = {pk2(o[0][j], o[1][j]), pk2(o[2][j], o[3][j]), pk2(o[4][j], o[5][j])};
    __builtin_amdgcn_raw_buffer_store_b96(x, rsO, ov + ((j & 3) + 8 * (j >> 2)) * ROWB, 0, 2);
  }
}

extern "C" void kernel_launch(void* const* d_in, const int* in_sizes, int n_in, void* d_out, int out_size, void* d_ws, size_t ws_size,
                              hipStream_t stream) {
  lin_kernel<<<256, 768, 0, stream>>>((const char*)d_in[0], (const char*)d_in[1], (const char*)d_in[2], (const char*)d_in[3],
                                      (const char*)d_in[4], (char*)d_ws, (char*)d_out);
}
```

```cpp
#include <hip/hip_runtime.h>
#include <hip/hip_bf16.h>

typedef short s16x4 __attribute__((ext_vector_type(4)));
typedef __bf16 bf16x8 __attribute__((ext_vector_type(8)));
typedef __bf16 bf16x2 __attribute__((ext_vector_type(2)));
typedef float f32x2 __attribute__((ext_vector_type(2)));
typedef float f32x4 __attribute__((ext_vector_type(4)));
typedef float f32x16 __attribute__((ext_vector_type(16)));
typedef unsigned u32x4 __attribute__((ext_vector_type(4)));
typedef unsigned u32x2 __attribute__((ext_vector_type(2)));
typedef unsigned u32x3 __attribute__((ext_vector_type(3)));
#define LAS __attribute__((address_space(3)))
#define GAS __attribute__((address_space(1)))
#define DI __device__ __forceinline__
#define RLX_AGENT __ATOMIC_RELAXED, __HIP_MEMORY_SCOPE_AGENT
#define RLX_WG __ATOMIC_RELAXED, __HIP_MEMORY_SCOPE_WORKGROUP
typedef GAS unsigned gu32;

#define RING 6
#define QPRE 1
#define POLLGAP 3
#define RETRY_SLEEP 32
#define W1 24
#define S2START 6
#define QSTART 4
#define W2 12
#define LATEPUB 20
#define QFEARLY 1
#define SMX 1
#define ISSAT 0
#define VDELAY 0
#define PRETOUCH 1
#define PSTEP 65536u
#define PDELAY 0
#define STAMPS 0

constexpr int H = 32, SQ = 2048, SKV = 2048, DH = 192, R = 64;
constexpr int ROWB = DH * 2;
constexpr int SLOT = 16384;
constexpr int NST = 24;
constexpr int PART_OFF = 49152;
constexpr int PAY_HEAD = 2 * 49152;
constexpr int CTL_OFF = H * PAY_HEAD;
constexpr int CTL_HEAD = 17 * 128;
constexpr int DBG_OFF = CTL_OFF + H * CTL_HEAD + 4096;
constexpr unsigned POISON = 0xFFFFFFFFu;
constexpr unsigned SPIN_CAP = 1u << 20;
constexpr int VT = 4096 + 16;
constexpr int L_KP = 0, L_VP = 24576, L_SCR_HI = 49152 + 256, L_SCR_LO = L_SCR_HI + 49152, L_FLAG = L_SCR_LO + 49152, L_BYTES = L_FLAG + 64;
constexpr int F_ISS = 0, F_KP = 1, F_VP = 2, F_Q = 3, F_GO = 4  , F_Q2 = 6  ;

template <bool B> struct BoolC { static constexpr bool value = B; };
DI unsigned pk2(float a, float b) {
  f32x2 f = {a, b};
  return __builtin_bit_cast(unsigned, __builtin_convertvector(f, bf16x2));
}
DI f32x4 mfma16(u32x4 a, u32x4 b, f32x4 c) {
  return __builtin_amdgcn_mfma_f32_16x16x32_bf16(__builtin_bit_cast(bf16x8, a), __builtin_bit_cast(bf16x8, b), c, 0, 0, 0);
}
DI f32x16 mfma32(u32x4 a, u32x4 b, f32x16 c) {
  return __builtin_amdgcn_mfma_f32_32x32x16_bf16(__builtin_bit_cast(bf16x8, a), __builtin_bit_cast(bf16x8, b), c, 0, 0, 0);
}
DI u32x4 tr2(LAS char* p0, LAS char* p1) {
  u32x2 l2 = __builtin_bit_cast(u32x2, __builtin_amdgcn_ds_read_tr16_b64_v4i16((LAS s16x4*)p0));
  u32x2 h2 = __builtin_bit_cast(u32x2, __builtin_amdgcn_ds_read_tr16_b64_v4i16((LAS s16x4*)p1));
  return u32x4{l2[0], l2[1], h2[0], h2[1]};
}
DI void dma_nt(unsigned lds, unsigned voff, __amdgpu_buffer_rsrc_t rs, unsigned soff) {
  unsigned keep;
  asm volatile("s_mov_b32 %0, m0\n\ts_mov_b32 m0, %1\n\ts_nop 0\n\tbuffer_load_dwordx4 %2, %3, %4 offen nt lds\n\ts_mov_b32 m0, %0"
               : "=&s"(keep) : "s"(lds), "v"(voff), "s"(rs), "s"(soff) : "memory");
}
DI void dma(unsigned lds, unsigned voff, __amdgpu_buffer_rsrc_t rs, unsigned soff) {
  unsigned keep;
  asm volatile("s_mov_b32 %0, m0\n\ts_mov_b32 m0, %1\n\ts_nop 0\n\tbuffer_load_dwordx4 %2, %3, %4 offen lds\n\ts_mov_b32 m0, %0"
               : "=&s"(keep) : "s"(lds), "v"(voff), "s"(rs), "s"(soff) : "memory");
}
template <int N> DI void vmcnt() { asm volatile("s_waitcnt vmcnt(%0)" ::"n"(N) : "memory"); }
DI void lgkm0() { asm volatile("s_waitcnt lgkmcnt(0)" ::: "memory"); }
DI void bar() {
  lgkm0();
  __builtin_amdgcn_s_barrier();
  asm volatile("" ::: "memory");
}
template <int N> DI void bars() {
#pragma unroll
  for (int i = 0; i < N; ++i) __builtin_amdgcn_s_barrier();
  asm volatile("" ::: "memory");
}
DI u32x4 ld_sc1(__amdgpu_buffer_rsrc_t rs, unsigned voff, unsigned soff) { return __builtin_amdgcn_raw_buffer_load_b128(rs, voff, soff, 16); }
DI void st_sc1(u32x4 v, __amdgpu_buffer_rsrc_t rs, unsigned voff) { __builtin_amdgcn_raw_buffer_store_b128(v, rs, voff, 0, 16); }
DI unsigned addbf2(unsigned x, unsigned y) {
  const float lo = __uint_as_float(x << 16) + __uint_as_float(y << 16);
  const float hi = __uint_as_float(x & 0xffff0000u) + __uint_as_float(y & 0xffff0000u);
  return pk2(lo, hi);
}
DI unsigned umin2(unsigned a, unsigned b) { return a < b ? a : b; }
DI unsigned umax2(unsigned a, unsigned b) { return a > b ? a : b; }
DI unsigned umax4(u32x4 v) { return umax2(umax2(v[0], v[1]), umax2(v[2], v[3])); }
DI bool has_poison(u32x4 v) { return __builtin_amdgcn_ballot_w64(umax4(v) == POISON) != 0ull; }
DI u32x4 clean(u32x4 v) { return u32x4{umin2(v[0], POISON - 1u), umin2(v[1], POISON - 1u), umin2(v[2], POISON - 1u), umin2(v[3], POISON - 1u)}; }
DI unsigned at_read(gu32* p) {
  unsigned z = 0u;
  asm volatile("" : "+v"(z));
  return __hip_atomic_fetch_add(p, z, RLX_AGENT);
}
DI void at_write(gu32* p, unsigned v) { asm volatile("global_atomic_swap %0, %1, off" ::"v"(p), "v"(v) : "memory"); }
DI void lds_wait(LAS unsigned* p, unsigned want) {
#pragma unroll 1
  for (unsigned spins = 0; spins < (SPIN_CAP << 6); ++spins) {
    if ((unsigned)__builtin_amdgcn_readfirstlane(__hip_atomic_load(p, RLX_WG)) >= want) break;
    __builtin_amdgcn_s_sleep(1);
  }
  asm volatile("" ::: "memory");
}
#define STAMP(i) do { } while (0)

__global__ void __launch_bounds__(768, 3) lin_kernel(const char* __restrict__ q, const char* __restrict__ k, const char* __restrict__ v,
                                                     const char* __restrict__ e, const char* __restrict__ f, char* ws,
                                                     char* __restrict__ out) {
  __shared__ __attribute__((aligned(1024))) char smem[L_BYTES];
  LAS char* L = (LAS char*)smem;
  LAS unsigned* flag = (LAS unsigned*)(L + L_FLAG);
  STAMP(0);
  const int b = blockIdx.x, xcd = b & 7, jj = b >> 3, head = xcd * 4 + (jj >> 3), sub = jj & 7;
  const int tensor = sub >> 2, role = sub & 3;
  const int tid = threadIdx.x, lane = tid & 63, w = __builtin_amdgcn_readfirstlane(tid >> 6);
  const bool helper = role == 3;
  const int lw = w & 3;
  const unsigned ldsb = (unsigned)(size_t)L;
  const __amdgpu_buffer_rsrc_t rsK =
      __builtin_amdgcn_make_buffer_rsrc((char*)(tensor ? v : k) + (size_t)head * SKV * ROWB, 0, SKV * ROWB, 0x00020000);
  const __amdgpu_buffer_rsrc_t rsE = __builtin_amdgcn_make_buffer_rsrc((char*)(tensor ? f : e), 0, R * SKV * 2, 0x00020000);
  unsigned sk = helper ? 1536u * ROWB : role * 128u, se = helper ? 3072u : 0u;
  int pairs = helper ? 16 : 0;
  unsigned voK[2], voE[2];
#pragma unroll
  for (int x = 0; x < 2; ++x) {
    const int row = 8 * (2 * lw + x) + (lane >> 3);
    voK[x] = row * ROWB + (((lane & 7) ^ (((lane >> 4) & 1) << 2)) << 4);
    voE[x] = row * (SKV * 2) + (((lane & 7) ^ ((x << 2) | (lane >> 4))) << 4);
  }
  unsigned dsl = ldsb + lw * 2048;
  auto issue = [&]() {
    dma_nt(dsl, voK[0], rsK, sk);
    dma_nt(dsl + 1024, voK[1], rsK, sk);
    dma(dsl + 8192, voE[0], rsE, se);
    dma(dsl + 9216, voE[1], rsE, se);
    if (pairs > 0) {
      --pairs;
      sk += pairs & 1 ? 128u : pairs ? 64u * ROWB - 128u : 128u - 448u * ROWB;
      se += pairs & 1 ? 0u : pairs ? 128u : -896u;
    } else {
      sk += 64u * ROWB;
      se += 128u;
    }
    dsl += SLOT;
    if (dsl >= ldsb + RING * SLOT) dsl -= RING * SLOT;
  };
  if ((w & 12) == 4) {
#pragma unroll
    for (int s = 0; s < RING - 1; ++s) issue();
  }
  __builtin_amdgcn_sched_barrier(0);
  const __amdgpu_buffer_rsrc_t rsP = __builtin_amdgcn_make_buffer_rsrc(ws + (size_t)head * PAY_HEAD, 0, PAY_HEAD, 0x00020000);
  gu32* ctl = (gu32*)(ws + CTL_OFF + head * CTL_HEAD);

  const __amdgpu_buffer_rsrc_t rsQ =
      __builtin_amdgcn_make_buffer_rsrc((char*)q + ((size_t)head * SQ + sub * 256) * ROWB, 0, 256 * ROWB, 0x00020000);
  unsigned voQ[6];
  auto qprep = [&]() {
#pragma unroll
    for (int bb = 0; bb < 3; ++bb) {
      const int idx = 64 * bb + lane, row = idx / 24, c = idx - row * 24;
      voQ[bb] = row * 384 + (((c & 24) | ((c & 7) ^ (row >> 1))) << 4);
      voQ[3 + bb] = (voQ[bb] ^ 64u) + 3072u;
    }
    asm volatile("" : "+v"(voQ[0]), "+v"(voQ[1]), "+v"(voQ[2]), "+v"(voQ[3]), "+v"(voQ[4]), "+v"(voQ[5]));
  };
  auto qgroup = [&](int j) {
    const unsigned d = ldsb + (j < 8 ? L_SCR_LO : L_SCR_HI - 49152) + j * 6144;
#pragma unroll
    for (int i = 0; i < 6; ++i) dma_nt(d + i * 1024, voQ[i], rsQ, j * 6144);
  };

  if (__builtin_expect(w == 8, 0)) {
    const int l8 = lane & 7;
    gu32* nonce_p = ctl + l8;
    gu32* inbox_p = ctl + (1 + 2 * sub + (l8 >> 2)) * 32 + (l8 & 3);
    gu32* note_p = ctl + (1 + 2 * l8 + tensor) * 32 + role;
    const unsigned old = __hip_atomic_load(lane < 8 ? inbox_p : ctl + sub, RLX_AGENT);
    if (lane < 8) flag[lane] = 0u;
    const u32x4 pz = {POISON, POISON, POISON, POISON};
#pragma unroll 1
    for (int s = helper ? 0 : 2; s < 3; ++s) {
      const unsigned base = lane * 16 + tensor * 24576 + (helper ? PART_OFF + s * 4096 : role * 4096);
#pragma unroll
      for (int i = 0; i < 8; ++i) st_sc1(pz, rsP, base + (i >> 2) * 12288 + (i & 3) * 1024);
    }
    lgkm0();
    bars<1>();
    unsigned n = __builtin_amdgcn_readlane(old, 8) + 1u;
    while (__builtin_amdgcn_ballot_w64(old == n) != 0ull) ++n;
    if (lane == 0) at_write(ctl + sub, n);
    bars<9>();
    unsigned echoed = __hip_atomic_load(nonce_p, RLX_AGENT);
    bars<6>();
    vmcnt<0>();
    asm volatile("" : "+v"(echoed));
    bars<8>();
    if (lane < 8) at_write(note_p, echoed);
    auto refresh = [&]() {
      const unsigned cur = __hip_atomic_load(nonce_p, RLX_AGENT);
      if (lane < 8 && cur != echoed) at_write(note_p, cur);
      echoed = cur;
    };
    bars<1>();
    unsigned seen = 0u;
#pragma unroll 1
    for (unsigned spins = 0; spins < (SPIN_CAP >> 3); ++spins) {
      if (lane < 8) {
        unsigned c0, c1, c2;
#define POLL_STEP(NEW, OLD)                                                                                                        \
  "global_atomic_add " NEW ", %4, %5, off sc0\n\ts_sleep %6\n\ts_waitcnt vmcnt(2)\n\tv_cmp_eq_u32 vcc, %9, " OLD "\n\t"           \
  "s_or_b32 %3, %3, vcc_lo\n\tv_and_b32 " OLD ", %3, %10\n\tv_cmp_eq_u32 vcc, " OLD ", %10\n\t"                                  \
  "s_and_saveexec_b64 s[6:7], vcc\n\tds_write_b32 %7, %8\n\ts_mov_b64 exec, s[6:7]\n\ts_cmp_eq_u32 %3, 0xff\n\ts_cbranch_scc1 2f\n\t"
        asm volatile(
            "global_atomic_add %0, %4, %5, off sc0\n\ts_sleep %6\n\t"
            "global_atomic_add %1, %4, %5, off sc0\n\ts_sleep %6\n\t"
            "s_mov_b32 s4, 7\n"
            "1:\n\t" POLL_STEP("%2", "%0") POLL_STEP("%0", "%1") POLL_STEP("%1", "%2")
            "s_sub_u32 s4, s4, 1\n\ts_cbranch_scc0 1b\n"
            "2:\n\t"
            "s_waitcnt vmcnt(0) lgkmcnt(0)"
            : "=&v"(c0), "=&v"(c1), "=&v"(c2), "+s"(seen)
            : "v"(inbox_p), "v"(0u), "n"(POLLGAP), "v"(ldsb + L_FLAG + (F_GO + (l8 >> 2)) * 4), "v"(1u), "s"(n), "v"(l8 & 4 ? 0xf0u : 0x0fu)
            : "memory", "vcc", "scc", "s4", "s6", "s7");
      }
      seen = __builtin_amdgcn_readfirstlane(seen);
      refresh();
      if (seen == 0xffu) break;
    }
    if (lane < 2) __hip_atomic_store(flag + F_GO + lane, 1u, RLX_WG);
    return;
  }
  if (__builtin_expect(w == 9, 0)) {
    {
      const __amdgpu_buffer_rsrc_t rsT =
          __builtin_amdgcn_make_buffer_rsrc(out + ((size_t)head * SQ + sub * 256) * ROWB, 0, 256 * ROWB, 0x00020000);
      if (PDELAY) __builtin_amdgcn_s_sleep(PDELAY);
      const unsigned k_lo = helper ? 1536u * ROWB : 0u, k_hi = helper ? SKV * ROWB - 16u : 1536u * ROWB - 16u;
      dma_nt(ldsb + L_SCR_LO, umin2(k_lo + lane * PSTEP, k_hi), rsK, 0);
      if (PRETOUCH > 1) {
        const unsigned tv = umin2(lane * PSTEP, 256 * ROWB - 16);
        dma_nt(ldsb + L_SCR_LO + 1024, tv, rsQ, 0);
        dma_nt(ldsb + L_SCR_LO + 2048, tv, rsT, 0);
      }
    }
    qprep();
    bars<NST + 1 - QSTART>();
    int j = 0;
#pragma unroll 1
    for (int i = 0; i < QSTART; ++i) {
#pragma unroll 1
      for (int x = 0; x < QPRE && j < 8; ++x, ++j) qgroup(j);
      bars<1>();
    }
#pragma unroll 1
    for (; j < 8; ++j) {
      if ((unsigned)__builtin_amdgcn_readfirstlane(__hip_atomic_load(flag + F_ISS, RLX_WG)) < 8u) {
        vmcnt<W1 - 6>();
        if (j + 1 - W1 / 6 > 0 && lane == 0) __hip_atomic_store(flag + F_Q, (unsigned)(j + 1 - W1 / 6), RLX_WG);
      }
      qgroup(j);
    }
#define QTAIL(F, N)                                                        \
  vmcnt<6 * (N)>();                                                        \
  if (lane == 0) __hip_atomic_store(flag + (F), 8u - (N), RLX_WG);
    vmcnt<36>();
    if (lane == 0) __hip_atomic_fetch_max(flag + F_Q, 2u, RLX_WG);
    vmcnt<24>();
    if (lane == 0) __hip_atomic_fetch_max(flag + F_Q, 4u, RLX_WG);
    QTAIL(F_Q, 3) QTAIL(F_Q, 2) QTAIL(F_Q, 1) QTAIL(F_Q, 0)
    return;
  }
  if (__builtin_expect(w >= 10, 0)) {
    qprep();
    bars<NST + 1>();
    bool wide = false;
#pragma unroll 1
    for (unsigned spins = 0; spins < (SPIN_CAP << 6); ++spins) {
      wide = (unsigned)__builtin_amdgcn_readfirstlane(__hip_atomic_load(flag + F_ISS, RLX_WG)) >= 8u;
      if (wide || (unsigned)__builtin_amdgcn_readfirstlane(__hip_atomic_load(flag + F_Q, RLX_WG)) >= S2START) break;
      __builtin_amdgcn_s_sleep(1);
    }
#pragma unroll 1
    for (int x = 0; x < 4; ++x) {
      if (!wide) {
        vmcnt<W2 - 6>();
        wide = (unsigned)__builtin_amdgcn_readfirstlane(__hip_atomic_load(flag + F_ISS, RLX_WG)) >= 8u;
      }
      qgroup(8 + 2 * (w - 10) + (x & 1) + 4 * (x >> 1));
    }
    vmcnt<12>();
    if (lane == 0) __hip_atomic_store(flag + F_Q2 + (w - 10), 1u, RLX_WG);
    vmcnt<0>();
    if (lane == 0) __hip_atomic_store(flag + F_Q2 + (w - 10), 2u, RLX_WG);
    return;
  }

  const int q4 = (lane & 15) >> 2, p4 = lane & 3, r32 = lane & 31, hh = lane >> 5;
  const int ch = w >> 2;
  const unsigned go = lw * 1024 + lane * 16, gs = ch * 24576;
  unsigned dst[6];
#pragma unroll
  for (int i = 0; i < 6; ++i) {
    const unsigned d = 64 * (i % 3) + 32 * (i / 3) + (r32 & 19) + ((r32 & 4) << 1) + ((r32 & 8) >> 1), d6 = d / 6u;
    dst[i] = ch ? L_VP + (d - 6 * d6) * VT + lw * 1024 + hh * 512 + d6 * 16 : L_KP + i * 4096 + go;
  }
  asm volatile("" : "+v"(dst[0]), "+v"(dst[1]), "+v"(dst[2]), "+v"(dst[3]), "+v"(dst[4]), "+v"(dst[5]));
  static_assert(RING >= 4 && RING <= 6, "ring depth");
  if (ch) {
#pragma unroll 1
    for (int s = 0; s < NST - (RING - 1); ++s) {
      vmcnt<4 * (RING - 2)>();
      bars<1>();
      issue();
    }
    if (RING > 5) { vmcnt<16>(); bars<1>(); }
    if (RING > 4) { vmcnt<12>(); bars<1>(); }
    vmcnt<8>(); bars<1>();
    vmcnt<4>(); bars<1>();
    vmcnt<0>(); bars<1>();
    STAMP(3);
  } else {
    const int rq = lw & 1, cq = lw >> 1;
    const unsigned kt = (8 * hh + q4) * 128 + ((cq ^ (q4 >> 1)) << 6) + ((lane >> 4) & 1) * 32 + (p4 & 1) * 16 + (p4 >> 1) * 8;
    unsigned et[4];
#pragma unroll
    for (int kk = 0; kk < 4; ++kk) et[kk] = 8192 + (32 * rq + r32) * 128 + (((2 * kk + hh) ^ ((r32 >> 1) & 7)) << 4);
    const unsigned fin_v = tensor ? 24576 + cq * 12288 + rq * 2048 + lane * 16 : rq * 12288 + cq * 2048 + lane * 16;
    f32x16 acc, aco;
#pragma unroll
    for (int i = 0; i < 16; ++i) { acc[i] = 0.f; aco[i] = 0.f; }
    auto pack = [&](const f32x16& x, int t) {
      return clean(u32x4{pk2(x[8 * t], x[8 * t + 1]), pk2(x[8 * t + 2], x[8 * t + 3]), pk2(x[8 * t + 4], x[8 * t + 5]), pk2(x[8 * t + 6], x[8 * t + 7])});
    };
    auto run = [&](auto ISV) {
      u32x4 ea[4], eb[4], oa[4], ob[4];
#pragma unroll
      for (int kk = 0; kk < 4; ++kk) { oa[kk] = u32x4{0u, 0u, 0u, 0u}; ob[kk] = u32x4{0u, 0u, 0u, 0u}; }
      int so = 0;
      auto mul = [&](f32x16& c, u32x4(&fa_)[4], u32x4(&fb_)[4]) {
#pragma unroll
        for (int kk = 0; kk < 4; ++kk) c = ISV.value ? mfma32(fb_[kk], fa_[kk], c) : mfma32(fa_[kk], fb_[kk], c);
      };
      auto stage = [&](u32x4(&na)[4], u32x4(&nb)[4], f32x16& c, u32x4(&fa_)[4], u32x4(&fb_)[4]) {
        bars<1>();
        LAS char* sl = L + so;
#pragma unroll
        for (int kk = 0; kk < 4; ++kk) {
          na[kk] = tr2(sl + kt + kk * 2048, sl + kt + kk * 2048 + 512);
          nb[kk] = *(LAS u32x4*)(sl + et[kk]);
        }
        __builtin_amdgcn_sched_barrier(0);
        mul(c, fa_, fb_);
        __builtin_amdgcn_sched_barrier(0);
        lgkm0();
        so += SLOT;
        if (so == RING * SLOT) so = 0;
      };
      u32x4 held[4];
#pragma unroll
      for (int i = 0; i < 4; ++i) held[i] = u32x4{0u, 0u, 0u, 0u};
#pragma unroll 1
      for (int s = 0; s < NST; s += 2) {
        stage(ea, eb, aco, oa, ob);
        if (helper && s == 16) {
          held[0] = pack(acc, 0); held[1] = pack(acc, 1); held[2] = pack(aco, 0); held[3] = pack(aco, 1);
#pragma unroll
          for (int i = 0; i < 16; ++i) { acc[i] = 0.f; aco[i] = 0.f; }
        }
        if (helper && s == LATEPUB) {
#pragma unroll
          for (int i = 0; i < 4; ++i) st_sc1(held[i], rsP, fin_v + PART_OFF + (i >> 1) * 4096 + (i & 1) * 1024);
        }
        stage(oa, ob, acc, ea, eb);
      }
      mul(aco, oa, ob);
    };
    if (tensor) run(BoolC<true>{});
    else run(BoolC<false>{});
    STAMP(3);
    acc += aco;
    const unsigned fin_o = fin_v + (helper ? PART_OFF + 8192 : role * 4096);
    st_sc1(pack(acc, 0), rsP, fin_o);
    st_sc1(pack(acc, 1), rsP, fin_o + 1024);
    asm volatile("" ::: "memory");
  }
  bar();
  STAMP(2);
  LAS char* scr = L + (w < 4 ? L_SCR_LO : L_SCR_HI) + lw * 12288;
  unsigned fa[4];
#pragma unroll
  for (int k4 = 0; k4 < 4; ++k4) fa[k4] = r32 * 384 + (((2 * k4 + hh) ^ ((r32 >> 1) & 7)) << 4);
  LAS unsigned* qflag = flag + (ch ? F_Q2 + (lw & 1) : F_Q);
  const unsigned qwant = ch ? (lw >> 1) + 1 : 2 * lw + 2;
  lds_wait(flag + F_GO + ch, 1u);
  if (VDELAY && ch) __builtin_amdgcn_s_sleep(VDELAY);
  STAMP(11);
  if (ISSAT == 0 && lane == 0) __hip_atomic_fetch_add(flag + F_ISS, 1u, RLX_WG);
#pragma unroll 1
  for (unsigned spins = 0; spins < SPIN_CAP; ++spins) {
    asm volatile("" ::: "memory");
    u32x4 gv[12];
#pragma unroll
    for (int i = 0; i < 12; ++i) gv[i] = ld_sc1(rsP, go, gs + (i >> 1) * 4096 + (i & 1) * PART_OFF);
    if (ISSAT == 1 && spins == 0 && lane == 0) __hip_atomic_fetch_add(flag + F_ISS, 1u, RLX_WG);
    __builtin_amdgcn_sched_barrier(0);
    unsigned mx = 0u;
#pragma unroll
    for (int i = 0; i < 6; ++i) {
      const u32x4 x = gv[2 * i], y = gv[2 * i + 1];
      mx = umax2(mx, umax2(umax4(x), umax4(y)));
      *(LAS u32x4*)(L + dst[i]) = u32x4{addbf2(x[0], y[0]), addbf2(x[1], y[1]), addbf2(x[2], y[2]), addbf2(x[3], y[3])};
      __builtin_amdgcn_sched_barrier(0);
    }
    if (__builtin_amdgcn_ballot_w64(mx == POISON) == 0ull) break;
    __builtin_amdgcn_s_sleep(RETRY_SLEEP);
  }
  lgkm0();
  if (lane == 0) __hip_atomic_fetch_add(flag + F_KP + ch, 1u, RLX_WG);
  STAMP(4);
  lds_wait(qflag, qwant);
  STAMP(5);
  u32x4 qf[12];
#pragma unroll
  for (int ks = 0; ks < 12; ++ks) qf[ks] = *(LAS u32x4*)(scr + fa[ks & 3] + (ks >> 2) * 128);
  lds_wait(flag + F_KP, 4u);
  STAMP(6);

  const float cs = 0.07216878364870322f * 1.4426950408889634f;
  auto xmax = [](float x) {
    auto rr = __builtin_amdgcn_permlane32_swap(__float_as_uint(x), __float_as_uint(x), false, false);
    return fmaxf(__uint_as_float(rr[0]), __uint_as_float(rr[1]));
  };
  f32x16 s0, s1;
#pragma unroll
  for (int i = 0; i < 16; ++i) { s0[i] = 0.f; s1[i] = 0.f; }
  float m0, sum0 = 0.f;
  {
    LAS u32x4* kf = (LAS u32x4*)(L + L_KP) + lane;
    u32x4 ka[24];
#pragma unroll
    for (int i = 0; i < 6; ++i) ka[i] = kf[i * 64];
    __builtin_amdgcn_sched_barrier(0);
#pragma unroll
    for (int i = 0; i < 12; ++i) {
      ka[i + 6] = kf[(i + 6) * 64];
      s0 = mfma32(ka[i], qf[i], s0);
      __builtin_amdgcn_sched_barrier(0);
    }
#pragma unroll
    for (int i = 12; i < 24; ++i) {
      if (i + 6 < 24) ka[i + 6] = kf[(i + 6) * 64];
      s1 = mfma32(ka[i], qf[i - 12], s1);
      if (i == 13) {
        m0 = s0[0];
#pragma unroll
        for (int j = 1; j < 8; ++j) m0 = fmaxf(m0, s0[j]);
      }
      if (i == 14) {
#pragma unroll
        for (int j = 8; j < 16; ++j) m0 = fmaxf(m0, s0[j]);
      }
      if (i == 15) m0 = xmax(m0) * cs;
      if (i >= 16) {
#pragma unroll
        for (int j = 2 * (i - 16); j < 2 * (i - 16) + 2; ++j) { s0[j] = __builtin_amdgcn_exp2f(__builtin_fmaf(s0[j], cs, -m0)); sum0 += s0[j]; }
      }
      __builtin_amdgcn_sched_barrier(0);
    }
  }
  float m = s1[0];
#pragma unroll
  for (int i = 1; i < 16; ++i) m = fmaxf(m, s1[i]);
  const float mc = fmaxf(xmax(m) * cs, m0);
  const float a0 = __builtin_amdgcn_exp2f(m0 - mc);
  float sum = sum0 * a0;
#pragma unroll
  for (int i = 0; i < 16; ++i) { s1[i] = __builtin_amdgcn_exp2f(__builtin_fmaf(s1[i], cs, -mc)); sum += s1[i]; }
  {
    auto rr = __builtin_amdgcn_permlane32_swap(__float_as_uint(sum), __float_as_uint(sum), false, false);
    sum = __uint_as_float(rr[0]) + __uint_as_float(rr[1]);
  }
  const float inv = __builtin_amdgcn_rcpf(sum), inv0 = inv * a0;
  u32x4 pf[4];
#pragma unroll
  for (int t = 0; t < 2; ++t) {
    pf[t] = u32x4{pk2(s0[8 * t] * inv0, s0[8 * t + 1] * inv0), pk2(s0[8 * t + 2] * inv0, s0[8 * t + 3] * inv0),
                  pk2(s0[8 * t + 4] * inv0, s0[8 * t + 5] * inv0), pk2(s0[8 * t + 6] * inv0, s0[8 * t + 7] * inv0)};
    pf[2 + t] = u32x4{pk2(s1[8 * t] * inv, s1[8 * t + 1] * inv), pk2(s1[8 * t + 2] * inv, s1[8 * t + 3] * inv),
                      pk2(s1[8 * t + 4] * inv, s1[8 * t + 5] * inv), pk2(s1[8 * t + 6] * inv, s1[8 * t + 7] * inv)};
  }
  STAMP(7);
  lds_wait(flag + F_VP, 4u);
  STAMP(8);
  f32x16 o[6];
#pragma unroll
  for (int t = 0; t < 6; ++t)
#pragma unroll
    for (int i = 0; i < 16; ++i) o[t][i] = 0.f;
  {
    LAS char* vb = L + L_VP + lane * 16;
    u32x4 va[24];
#pragma unroll
    for (int i = 0; i < 6; ++i) va[i] = *(LAS u32x4*)(vb + (i % 6) * VT + (i / 6) * 1024);
    __builtin_amdgcn_sched_barrier(0);
#pragma unroll
    for (int i = 0; i < 24; ++i) {
      if (i + 6 < 24) va[i + 6] = *(LAS u32x4*)(vb + ((i + 6) % 6) * VT + ((i + 6) / 6) * 1024);
      o[i % 6] = mfma32(pf[i / 6], va[i], o[i % 6]);
      __builtin_amdgcn_sched_barrier(0);
    }
  }
  const __amdgpu_buffer_rsrc_t rsO =
      __builtin_amdgcn_make_buffer_rsrc(out + ((size_t)head * SQ + sub * 256 + w * 32) * ROWB, 0, 32 * ROWB, 0x00020000);
  const unsigned ov = hh * 4 * ROWB + r32 * 12;
#pragma unroll
  for (int j = 0; j < 16; ++j) {
    const u32x3 x = {pk2(o[0][j], o[1][j]), pk2(o[2][j], o[3][j]), pk2(o[4][j], o[5][j])};
    __builtin_amdgcn_raw_buffer_store_b96(x, rsO, ov + ((j & 3) + 8 * (j >> 2)) * ROWB, 0, 2);
  }
}

extern "C" void kernel_launch(void* const* d_in, const int* in_sizes, int n_in, void* d_out, int out_size, void* d_ws, size_t ws_size,
                              hipStream_t stream) {
  lin_kernel<<<256, 768, 0, stream>>>((const char*)d_in[0], (const char*)d_in[1], (const char*)d_in[2], (const char*)d_in[3],
                                      (const char*)d_in[4], (char*)d_ws, (char*)d_out);
}
```

```cpp
#include <hip/hip_runtime.h>
#include <hip/hip_bf16.h>

typedef short s16x4 __attribute__((ext_vector_type(4)));
typedef __bf16 bf16x8 __attribute__((ext_vector_type(8)));
typedef __bf16 bf16x2 __attribute__((ext_vector_type(2)));
typedef float f32x2 __attribute__((ext_vector_type(2)));
typedef float f32x16 __attribute__((ext_vector_type(16)));
typedef unsigned u32x4 __attribute__((ext_vector_type(4)));
typedef unsigned u32x2 __attribute__((ext_vector_type(2)));
typedef unsigned u32x3 __attribute__((ext_vector_type(3)));
#define LAS __attribute__((address_space(3)))
#define GAS __attribute__((address_space(1)))
#define DI __device__ __forceinline__
#define RLX_AGENT __ATOMIC_RELAXED, __HIP_MEMORY_SCOPE_AGENT
#define RLX_WG __ATOMIC_RELAXED, __HIP_MEMORY_SCOPE_WORKGROUP
typedef GAS unsigned gu32;

constexpr int H = 32, SQ = 2048, SKV = 2048, DH = 192, R = 64;
constexpr int ROWB = DH * 2;
constexpr int SLOT = 16384;
constexpr int RING = 6, NST = 24;
constexpr int QSTART = 4;
constexpr int W1 = 24, W2 = 12, S2START = 6;
constexpr int LATEPUB = 20;
constexpr int POLLGAP = 3, RETRY_SLEEP = 32;
constexpr int PART_OFF = 49152;
constexpr int PAY_HEAD = 2 * 49152;
constexpr int CTL_OFF = H * PAY_HEAD;
constexpr int CTL_HEAD = 17 * 128;
constexpr unsigned POISON = 0xFFFFFFFFu;
constexpr unsigned SPIN_CAP = 1u << 20;
constexpr int VT = 4096 + 16;
constexpr int L_KP = 0, L_VP = 24576, L_SCR_HI = 49152 + 256, L_SCR_LO = L_SCR_HI + 49152, L_FLAG = L_SCR_LO + 49152, L_BYTES = L_FLAG + 64;
constexpr int F_ISS = 0, F_KP = 1, F_VP = 2, F_Q = 3, F_GO = 4  , F_Q2 = 6  ;

template <bool B> struct BoolC { static constexpr bool value = B; };
DI unsigned pk2(float a, float b) {
  f32x2 f = {a, b};
  return __builtin_bit_cast(unsigned, __builtin_convertvector(f, bf16x2));
}
DI f32x16 mfma32(u32x4 a, u32x4 b, f32x16 c) {
  return __builtin_amdgcn_mfma_f32_32x32x16_bf16(__builtin_bit_cast(bf16x8, a), __builtin_bit_cast(bf16x8, b), c, 0, 0, 0);
}
DI u32x4 tr2(LAS char* p0, LAS char* p1) {
  u32x2 l2 = __builtin_bit_cast(u32x2, __builtin_amdgcn_ds_read_tr16_b64_v4i16((LAS s16x4*)p0));
  u32x2 h2 = __builtin_bit_cast(u32x2, __builtin_amdgcn_ds_read_tr16_b64_v4i16((LAS s16x4*)p1));
  return u32x4{l2[0], l2[1], h2[0], h2[1]};
}
DI void dma_nt(unsigned lds, unsigned voff, __amdgpu_buffer_rsrc_t rs, unsigned soff) {
  unsigned keep;
  asm volatile("s_mov_b32 %0, m0\n\ts_mov_b32 m0, %1\n\ts_nop 0\n\tbuffer_load_dwordx4 %2, %3, %4 offen nt lds\n\ts_mov_b32 m0, %0"
               : "=&s"(keep) : "s"(lds), "v"(voff), "s"(rs), "s"(soff) : "memory");
}
DI void dma(unsigned lds, unsigned voff, __amdgpu_buffer_rsrc_t rs, unsigned soff) {
  unsigned keep;
  asm volatile("s_mov_b32 %0, m0\n\ts_mov_b32 m0, %1\n\ts_nop 0\n\tbuffer_load_dwordx4 %2, %3, %4 offen lds\n\ts_mov_b32 m0, %0"
               : "=&s"(keep) : "s"(lds), "v"(voff), "s"(rs), "s"(soff) : "memory");
}
template <int N> DI void vmcnt() { asm volatile("s_waitcnt vmcnt(%0)" ::"n"(N) : "memory"); }
DI void lgkm0() { asm volatile("s_waitcnt lgkmcnt(0)" ::: "memory"); }
DI void bar() {
  lgkm0();
  __builtin_amdgcn_s_barrier();
  asm volatile("" ::: "memory");
}
template <int N> DI void bars() {
#pragma unroll
  for (int i = 0; i < N; ++i) __builtin_amdgcn_s_barrier();
  asm volatile("" ::: "memory");
}
DI u32x4 ld_sc1(__amdgpu_buffer_rsrc_t rs, unsigned voff, unsigned soff) { return __builtin_amdgcn_raw_buffer_load_b128(rs, voff, soff, 16); }
DI void st_sc1(u32x4 v, __amdgpu_buffer_rsrc_t rs, unsigned voff) { __builtin_amdgcn_raw_buffer_store_b128(v, rs, voff, 0, 16); }
DI unsigned addbf2(unsigned x, unsigned y) {
  const float lo = __uint_as_float(x << 16) + __uint_as_float(y << 16);
  const float hi = __uint_as_float(x & 0xffff0000u) + __uint_as_float(y & 0xffff0000u);
  return pk2(lo, hi);
}
DI unsigned umin2(unsigned a, unsigned b) { return a < b ? a : b; }
DI unsigned umax2(unsigned a, unsigned b) { return a > b ? a : b; }
DI unsigned umax4(u32x4 v) { return umax2(umax2(v[0], v[1]), umax2(v[2], v[3])); }
DI u32x4 clean(u32x4 v) { return u32x4{umin2(v[0], POISON - 1u), umin2(v[1], POISON - 1u), umin2(v[2], POISON - 1u), umin2(v[3], POISON - 1u)}; }
DI void at_write(gu32* p, unsigned v) { asm volatile("global_atomic_swap %0, %1, off" ::"v"(p), "v"(v) : "memory"); }
DI void lds_wait(LAS unsigned* p, unsigned want) {
#pragma unroll 1
  for (unsigned spins = 0; spins < (SPIN_CAP << 6); ++spins) {
    if ((unsigned)__builtin_amdgcn_readfirstlane(__hip_atomic_load(p, RLX_WG)) >= want) break;
    __builtin_amdgcn_s_sleep(1);
  }
  asm volatile("" ::: "memory");
}

__global__ void __launch_bounds__(768, 3) lin_kernel(const char* __restrict__ q, const char* __restrict__ k, const char* __restrict__ v,
                                                     const char* __restrict__ e, const char* __restrict__ f, char* ws,
                                                     char* __restrict__ out) {
  __shared__ __attribute__((aligned(1024))) char smem[L_BYTES];
  LAS char* L = (LAS char*)smem;
  LAS unsigned* flag = (LAS unsigned*)(L + L_FLAG);
  const int b = blockIdx.x, xcd = b & 7, jj = b >> 3, head = xcd * 4 + (jj >> 3), sub = jj & 7;
  const int tensor = sub >> 2, role = sub & 3;
  const int tid = threadIdx.x, lane = tid & 63, w = __builtin_amdgcn_readfirstlane(tid >> 6);
  const bool helper = role == 3;
  const int lw = w & 3;
  const unsigned ldsb = (unsigned)(size_t)L;
  const __amdgpu_buffer_rsrc_t rsK =
      __builtin_amdgcn_make_buffer_rsrc((char*)(tensor ? v : k) + (size_t)head * SKV * ROWB, 0, SKV * ROWB, 0x00020000);
  const __amdgpu_buffer_rsrc_t rsE = __builtin_amdgcn_make_buffer_rsrc((char*)(tensor ? f : e), 0, R * SKV * 2, 0x00020000);
  unsigned sk = helper ? 1536u * ROWB : role * 128u, se = helper ? 3072u : 0u;
  int pairs = helper ? 16 : 0;
  unsigned voK[2], voE[2];
#pragma unroll
  for (int x = 0; x < 2; ++x) {
    const int row = 8 * (2 * lw + x) + (lane >> 3);
    voK[x] = row * ROWB + (((lane & 7) ^ (((lane >> 4) & 1) << 2)) << 4);
    voE[x] = row * (SKV * 2) + (((lane & 7) ^ ((x << 2) | (lane >> 4))) << 4);
  }
  unsigned dsl = ldsb + lw * 2048;
  auto issue = [&]() {
    dma_nt(dsl, voK[0], rsK, sk);
    dma_nt(dsl + 1024, voK[1], rsK, sk);
    dma(dsl + 8192, voE[0], rsE, se);
    dma(dsl + 9216, voE[1], rsE, se);
    if (pairs > 0) {
      --pairs;
      sk += pairs & 1 ? 128u : pairs ? 64u * ROWB - 128u : 128u - 448u * ROWB;
      se += pairs & 1 ? 0u : pairs ? 128u : -896u;
    } else {
      sk += 64u * ROWB;
      se += 128u;
    }
    dsl += SLOT;
    if (dsl >= ldsb + RING * SLOT) dsl -= RING * SLOT;
  };
  if ((w & 12) == 4) {
#pragma unroll
    for (int s = 0; s < RING - 1; ++s) issue();
  }
  __builtin_amdgcn_sched_barrier(0);
  const __amdgpu_buffer_rsrc_t rsP = __builtin_amdgcn_make_buffer_rsrc(ws + (size_t)head * PAY_HEAD, 0, PAY_HEAD, 0x00020000);
  gu32* ctl = (gu32*)(ws + CTL_OFF + head * CTL_HEAD);

  const __amdgpu_buffer_rsrc_t rsQ =
      __builtin_amdgcn_make_buffer_rsrc((char*)q + ((size_t)head * SQ + sub * 256) * ROWB, 0, 256 * ROWB, 0x00020000);
  unsigned voQ[6];
  auto qprep = [&]() {
#pragma unroll
    for (int bb = 0; bb < 3; ++bb) {
      const int idx = 64 * bb + lane, row = idx / 24, c = idx - row * 24;
      voQ[bb] = row * 384 + (((c & 24) | ((c & 7) ^ (row >> 1))) << 4);
      voQ[3 + bb] = (voQ[bb] ^ 64u) + 3072u;
    }
    asm volatile("" : "+v"(voQ[0]), "+v"(voQ[1]), "+v"(voQ[2]), "+v"(voQ[3]), "+v"(voQ[4]), "+v"(voQ[5]));
  };
  auto qgroup = [&](int j) {
    const unsigned d = ldsb + (j < 8 ? L_SCR_LO : L_SCR_HI - 49152) + j * 6144;
#pragma unroll
    for (int i = 0; i < 6; ++i) dma_nt(d + i * 1024, voQ[i], rsQ, j * 6144);
  };

  if (__builtin_expect(w == 8, 0)) {
    const int l8 = lane & 7;
    gu32* nonce_p = ctl + l8;
    gu32* inbox_p = ctl + (1 + 2 * sub + (l8 >> 2)) * 32 + (l8 & 3);
    gu32* note_p = ctl + (1 + 2 * l8 + tensor) * 32 + role;
    const unsigned old = __hip_atomic_load(lane < 8 ? inbox_p : ctl + sub, RLX_AGENT);
    if (lane < 8) flag[lane] = 0u;
    const u32x4 pz = {POISON, POISON, POISON, POISON};
#pragma unroll 1
    for (int s = helper ? 0 : 2; s < 3; ++s) {
      const unsigned base = lane * 16 + tensor * 24576 + (helper ? PART_OFF + s * 4096 : role * 4096);
#pragma unroll
      for (int i = 0; i < 8; ++i) st_sc1(pz, rsP, base + (i >> 2) * 12288 + (i & 3) * 1024);
    }
    lgkm0();
    bars<1>();
    unsigned n = __builtin_amdgcn_readlane(old, 8) + 1u;
    while (__builtin_amdgcn_ballot_w64(old == n) != 0ull) ++n;
    if (lane == 0) at_write(ctl + sub, n);
    bars<9>();
    unsigned echoed = __hip_atomic_load(nonce_p, RLX_AGENT);
    bars<6>();
    vmcnt<0>();
    asm volatile("" : "+v"(echoed));
    bars<8>();
    if (lane < 8) at_write(note_p, echoed);
    auto refresh = [&]() {
      const unsigned cur = __hip_atomic_load(nonce_p, RLX_AGENT);
      if (lane < 8 && cur != echoed) at_write(note_p, cur);
      echoed = cur;
    };
    bars<1>();
    unsigned seen = 0u;
#pragma unroll 1
    for (unsigned spins = 0; spins < (SPIN_CAP >> 3); ++spins) {
      if (lane < 8) {
        unsigned c0, c1, c2;
#define POLL_STEP(NEW, OLD)                                                                                                        \
  "global_atomic_add " NEW ", %4, %5, off sc0\n\ts_sleep %6\n\ts_waitcnt vmcnt(2)\n\tv_cmp_eq_u32 vcc, %9, " OLD "\n\t"           \
  "s_or_b32 %3, %3, vcc_lo\n\tv_and_b32 " OLD ", %3, %10\n\tv_cmp_eq_u32 vcc, " OLD ", %10\n\t"                                  \
  "s_and_saveexec_b64 s[6:7], vcc\n\tds_write_b32 %7, %8\n\ts_mov_b64 exec, s[6:7]\n\ts_cmp_eq_u32 %3, 0xff\n\ts_cbranch_scc1 2f\n\t"
        asm volatile(
            "global_atomic_add %0, %4, %5, off sc0\n\ts_sleep %6\n\t"
            "global_atomic_add %1, %4, %5, off sc0\n\ts_sleep %6\n\t"
            "s_mov_b32 s4, 7\n"
            "1:\n\t" POLL_STEP("%2", "%0") POLL_STEP("%0", "%1") POLL_STEP("%1", "%2")
            "s_sub_u32 s4, s4, 1\n\ts_cbranch_scc0 1b\n"
            "2:\n\t"
            "s_waitcnt vmcnt(0) lgkmcnt(0)"
            : "=&v"(c0), "=&v"(c1), "=&v"(c2), "+s"(seen)
            : "v"(inbox_p), "v"(0u), "n"(POLLGAP), "v"(ldsb + L_FLAG + (F_GO + (l8 >> 2)) * 4), "v"(1u), "s"(n), "v"(l8 & 4 ? 0xf0u : 0x0fu)
            : "memory", "vcc", "scc", "s4", "s6", "s7");
      }
      seen = __builtin_amdgcn_readfirstlane(seen);
      refresh();
      if (seen == 0xffu) break;
    }
    if (lane < 2) __hip_atomic_store(flag + F_GO + lane, 1u, RLX_WG);
    return;
  }
  if (__builtin_expect(w == 9, 0)) {
    qprep();
    bars<NST + 1 - QSTART>();
    int j = 0;
#pragma unroll 1
    for (int i = 0; i < QSTART; ++i) {
      qgroup(j++);
      bars<1>();
    }
#pragma unroll 1
    for (; j < 8; ++j) {
      if ((unsigned)__builtin_amdgcn_readfirstlane(__hip_atomic_load(flag + F_ISS, RLX_WG)) < 8u) {
        vmcnt<W1 - 6>();
        if (j + 1 - W1 / 6 > 0 && lane == 0) __hip_atomic_store(flag + F_Q, (unsigned)(j + 1 - W1 / 6), RLX_WG);
      }
      qgroup(j);
    }
#define QTAIL(F, N)                                                        \
  vmcnt<6 * (N)>();                                                        \
  if (lane == 0) __hip_atomic_store(flag + (F), 8u - (N), RLX_WG);
    vmcnt<36>();
    if (lane == 0) __hip_atomic_fetch_max(flag + F_Q, 2u, RLX_WG);
    vmcnt<24>();
    if (lane == 0) __hip_atomic_fetch_max(flag + F_Q, 4u, RLX_WG);
    QTAIL(F_Q, 3) QTAIL(F_Q, 2) QTAIL(F_Q, 1) QTAIL(F_Q, 0)
    return;
  }
  if (__builtin_expect(w >= 10, 0)) {
    qprep();
    bars<NST + 1>();
    bool wide = false;
#pragma unroll 1
    for (unsigned spins = 0; spins < (SPIN_CAP << 6); ++spins) {
      wide = (unsigned)__builtin_amdgcn_readfirstlane(__hip_atomic_load(flag + F_ISS, RLX_WG)) >= 8u;
      if (wide || (unsigned)__builtin_amdgcn_readfirstlane(__hip_atomic_load(flag + F_Q, RLX_WG)) >= S2START) break;
      __builtin_amdgcn_s_sleep(1);
    }
#pragma unroll 1
    for (int x = 0; x < 4; ++x) {
      if (!wide) {
        vmcnt<W2 - 6>();
        wide = (unsigned)__builtin_amdgcn_readfirstlane(__hip_atomic_load(flag + F_ISS, RLX_WG)) >= 8u;
      }
      qgroup(8 + 2 * (w - 10) + (x & 1) + 4 * (x >> 1));
    }
    vmcnt<12>();
    if (lane == 0) __hip_atomic_store(flag + F_Q2 + (w - 10), 1u, RLX_WG);
    vmcnt<0>();
    if (lane == 0) __hip_atomic_store(flag + F_Q2 + (w - 10), 2u, RLX_WG);
    return;
  }

  const int q4 = (lane & 15) >> 2, p4 = lane & 3, r32 = lane & 31, hh = lane >> 5;
  const int ch = w >> 2;
  const unsigned go = lw * 1024 + lane * 16, gs = ch * 24576;
  unsigned dst[6];
#pragma unroll
  for (int i = 0; i < 6; ++i) {
    const unsigned d = 64 * (i % 3) + 32 * (i / 3) + (r32 & 19) + ((r32 & 4) << 1) + ((r32 & 8) >> 1), d6 = d / 6u;
    dst[i] = ch ? L_VP + (d - 6 * d6) * VT + lw * 1024 + hh * 512 + d6 * 16 : L_KP + i * 4096 + go;
  }
  asm volatile("" : "+v"(dst[0]), "+v"(dst[1]), "+v"(dst[2]), "+v"(dst[3]), "+v"(dst[4]), "+v"(dst[5]));
  static_assert(RING >= 4 && RING <= 6, "ring depth");
  if (ch) {
#pragma unroll 1
    for (int s = 0; s < NST - (RING - 1); ++s) {
      vmcnt<4 * (RING - 2)>();
      bars<1>();
      issue();
    }
    if (RING > 5) { vmcnt<16>(); bars<1>(); }
    if (RING > 4) { vmcnt<12>(); bars<1>(); }
    vmcnt<8>(); bars<1>();
    vmcnt<4>(); bars<1>();
    vmcnt<0>(); bars<1>();
  } else {
    const int rq = lw & 1, cq = lw >> 1;
    const unsigned kt = (8 * hh + q4) * 128 + ((cq ^ (q4 >> 1)) << 6) + ((lane >> 4) & 1) * 32 + (p4 & 1) * 16 + (p4 >> 1) * 8;
    unsigned et[4];
#pragma unroll
    for (int kk = 0; kk < 4; ++kk) et[kk] = 8192 + (32 * rq + r32) * 128 + (((2 * kk + hh) ^ ((r32 >> 1) & 7)) << 4);
    const unsigned fin_v = tensor ? 24576 + cq * 12288 + rq * 2048 + lane * 16 : rq * 12288 + cq * 2048 + lane * 16;
    f32x16 acc, aco;
#pragma unroll
    for (int i = 0; i < 16; ++i) { acc[i] = 0.f; aco[i] = 0.f; }
    auto pack = [&](const f32x16& x, int t) {
      return clean(u32x4{pk2(x[8 * t], x[8 * t + 1]), pk2(x[8 * t + 2], x[8 * t + 3]), pk2(x[8 * t + 4], x[8 * t + 5]), pk2(x[8 * t + 6], x[8 * t + 7])});
    };
    auto run = [&](auto ISV) {
      u32x4 ea[4], eb[4], oa[4], ob[4];
#pragma unroll
      for (int kk = 0; kk < 4; ++kk) { oa[kk] = u32x4{0u, 0u, 0u, 0u}; ob[kk] = u32x4{0u, 0u, 0u, 0u}; }
      int so = 0;
      auto mul = [&](f32x16& c, u32x4(&fa_)[4], u32x4(&fb_)[4]) {
#pragma unroll
        for (int kk = 0; kk < 4; ++kk) c = ISV.value ? mfma32(fb_[kk], fa_[kk], c) : mfma32(fa_[kk], fb_[kk], c);
      };
      auto stage = [&](u32x4(&na)[4], u32x4(&nb)[4], f32x16& c, u32x4(&fa_)[4], u32x4(&fb_)[4]) {
        bars<1>();
        LAS char* sl = L + so;
#pragma unroll
        for (int kk = 0; kk < 4; ++kk) {
          na[kk] = tr2(sl + kt + kk * 2048, sl + kt + kk * 2048 + 512);
          nb[kk] = *(LAS u32x4*)(sl + et[kk]);
        }
        __builtin_amdgcn_sched_barrier(0);
        mul(c, fa_, fb_);
        __builtin_amdgcn_sched_barrier(0);
        lgkm0();
        so += SLOT;
        if (so == RING * SLOT) so = 0;
      };
      u32x4 held[4];
#pragma unroll
      for (int i = 0; i < 4; ++i) held[i] = u32x4{0u, 0u, 0u, 0u};
#pragma unroll 1
      for (int s = 0; s < NST; s += 2) {
        stage(ea, eb, aco, oa, ob);
        if (helper && s == 16) {
          held[0] = pack(acc, 0); held[1] = pack(acc, 1); held[2] = pack(aco, 0); held[3] = pack(aco, 1);
#pragma unroll
          for (int i = 0; i < 16; ++i) { acc[i] = 0.f; aco[i] = 0.f; }
        }
        if (helper && s == LATEPUB) {
#pragma unroll
          for (int i = 0; i < 4; ++i) st_sc1(held[i], rsP, fin_v + PART_OFF + (i >> 1) * 4096 + (i & 1) * 1024);
        }
        stage(oa, ob, acc, ea, eb);
      }
      mul(aco, oa, ob);
    };
    if (tensor) run(BoolC<true>{});
    else run(BoolC<false>{});
    acc += aco;
    const unsigned fin_o = fin_v + (helper ? PART_OFF + 8192 : role * 4096);
    st_sc1(pack(acc, 0), rsP, fin_o);
    st_sc1(pack(acc, 1), rsP, fin_o + 1024);
    asm volatile("" ::: "memory");
  }
  bar();
  LAS char* scr = L + (w < 4 ? L_SCR_LO : L_SCR_HI) + lw * 12288;
  unsigned fa[4];
#pragma unroll
  for (int k4 = 0; k4 < 4; ++k4) fa[k4] = r32 * 384 + (((2 * k4 + hh) ^ ((r32 >> 1) & 7)) << 4);
  LAS unsigned* qflag = flag + (ch ? F_Q2 + (lw & 1) : F_Q);
  const unsigned qwant = ch ? (lw >> 1) + 1 : 2 * lw + 2;
  lds_wait(flag + F_GO + ch, 1u);
  if (lane == 0) __hip_atomic_fetch_add(flag + F_ISS, 1u, RLX_WG);
#pragma unroll 1
  for (unsigned spins = 0; spins < SPIN_CAP; ++spins) {
    asm volatile("" ::: "memory");
    u32x4 gv[12];
#pragma unroll
    for (int i = 0; i < 12; ++i) gv[i] = ld_sc1(rsP, go, gs + (i >> 1) * 4096 + (i & 1) * PART_OFF);
    __builtin_amdgcn_sched_barrier(0);
    unsigned mx = 0u;
#pragma unroll
    for (int i = 0; i < 6; ++i) {
      const u32x4 x = gv[2 * i], y = gv[2 * i + 1];
      mx = umax2(mx, umax2(umax4(x), umax4(y)));
      *(LAS u32x4*)(L + dst[i]) = u32x4{addbf2(x[0], y[0]), addbf2(x[1], y[1]), addbf2(x[2], y[2]), addbf2(x[3], y[3])};
      __builtin_amdgcn_sched_barrier(0);
    }
    if (__builtin_amdgcn_ballot_w64(mx == POISON) == 0ull) break;
    __builtin_amdgcn_s_sleep(RETRY_SLEEP);
  }
  lgkm0();
  if (lane == 0) __hip_atomic_fetch_add(flag + F_KP + ch, 1u, RLX_WG);
  lds_wait(qflag, qwant);
  u32x4 qf[12];
#pragma unroll
  for (int ks = 0; ks < 12; ++ks) qf[ks] = *(LAS u32x4*)(scr + fa[ks & 3] + (ks >> 2) * 128);
  lds_wait(flag + F_KP, 4u);

  const float cs = 0.07216878364870322f * 1.4426950408889634f;
  auto xmax = [](float x) {
    auto rr = __builtin_amdgcn_permlane32_swap(__float_as_uint(x), __float_as_uint(x), false, false);
    return fmaxf(__uint_as_float(rr[0]), __uint_as_float(rr[1]));
  };
  f32x16 s0, s1;
#pragma unroll
  for (int i = 0; i < 16; ++i) { s0[i] = 0.f; s1[i] = 0.f; }
  float m0, sum0 = 0.f;
  {
    LAS u32x4* kf = (LAS u32x4*)(L + L_KP) + lane;
    u32x4 ka[24];
#pragma unroll
    for (int i = 0; i < 6; ++i) ka[i] = kf[i * 64];
    __builtin_amdgcn_sched_barrier(0);
#pragma unroll
    for (int i = 0; i < 12; ++i) {
      ka[i + 6] = kf[(i + 6) * 64];
      s0 = mfma32(ka[i], qf[i], s0);
      __builtin_amdgcn_sched_barrier(0);
    }
#pragma unroll
    for (int i = 12; i < 24; ++i) {
      if (i + 6 < 24) ka[i + 6] = kf[(i + 6) * 64];
      s1 = mfma32(ka[i], qf[i - 12], s1);
      if (i == 13) {
        m0 = s0[0];
#pragma unroll
        for (int j = 1; j < 8; ++j) m0 = fmaxf(m0, s0[j]);
      }
      if (i == 14) {
#pragma unroll
        for (int j = 8; j < 16; ++j) m0 = fmaxf(m0, s0[j]);
      }
      if (i == 15) m0 = xmax(m0) * cs;
      if (i >= 16) {
#pragma unroll
        for (int j = 2 * (i - 16); j < 2 * (i - 16) + 2; ++j) { s0[j] = __builtin_amdgcn_exp2f(__builtin_fmaf(s0[j], cs, -m0)); sum0 += s0[j]; }
      }
      __builtin_amdgcn_sched_barrier(0);
    }
  }
  float m = s1[0];
#pragma unroll
  for (int i = 1; i < 16; ++i) m = fmaxf(m, s1[i]);
  const float mc = fmaxf(xmax(m) * cs, m0);
  const float a0 = __builtin_amdgcn_exp2f(m0 - mc);
  float sum = sum0 * a0;
#pragma unroll
  for (int i = 0; i < 16; ++i) { s1[i] = __builtin_amdgcn_exp2f(__builtin_fmaf(s1[i], cs, -mc)); sum += s1[i]; }
  {
    auto rr = __builtin_amdgcn_permlane32_swap(__float_as_uint(sum), __float_as_uint(sum), false, false);
    sum = __uint_as_float(rr[0]) + __uint_as_float(rr[1]);
  }
  const float inv = __builtin_amdgcn_rcpf(sum), inv0 = inv * a0;
  u32x4 pf[4];
#pragma unroll
  for (int t = 0; t < 2; ++t) {
    pf[t] = u32x4{pk2(s0[8 * t] * inv0, s0[8 * t + 1] * inv0), pk2(s0[8 * t + 2] * inv0, s0[8 * t + 3] * inv0),
                  pk2(s0[8 * t + 4] * inv0, s0[8 * t + 5] * inv0), pk2(s0[8 * t + 6] * inv0, s0[8 * t + 7] * inv0)};
    pf[2 + t] = u32x4{pk2(s1[8 * t] * inv, s1[8 * t + 1] * inv), pk2(s1[8 * t + 2] * inv, s1[8 * t + 3] * inv),
                      pk2(s1[8 * t + 4] * inv, s1[8 * t + 5] * inv), pk2(s1[8 * t + 6] * inv, s1[8 * t + 7] * inv)};
  }
  lds_wait(flag + F_VP, 4u);
  f32x16 o[6];
#pragma unroll
  for (int t = 0; t < 6; ++t)
#pragma unroll
    for (int i = 0; i < 16; ++i) o[t][i] = 0.f;
  {
    LAS char* vb = L + L_VP + lane * 16;
    u32x4 va[24];
#pragma unroll
    for (int i = 0; i < 6; ++i) va[i] = *(LAS u32x4*)(vb + (i % 6) * VT + (i / 6) * 1024);
    __builtin_amdgcn_sched_barrier(0);
#pragma unroll
    for (int i = 0; i < 24; ++i) {
      if (i + 6 < 24) va[i + 6] = *(LAS u32x4*)(vb + ((i + 6) % 6) * VT + ((i + 6) / 6) * 1024);
      o[i % 6] = mfma32(pf[i / 6], va[i], o[i % 6]);
      __builtin_amdgcn_sched_barrier(0);
    }
  }
  const __amdgpu_buffer_rsrc_t rsO =
      __builtin_amdgcn_make_buffer_rsrc(out + ((size_t)head * SQ + sub * 256 + w * 32) * ROWB, 0, 32 * ROWB, 0x00020000);
  const unsigned ov = hh * 4 * ROWB + r32 * 12;
#pragma unroll
  for (int j = 0; j < 16; ++j) {
    const u32x3 x = {pk2(o[0][j], o[1][j]), pk2(o[2][j], o[3][j]), pk2(o[4][j], o[5][j])};
    __builtin_amdgcn_raw_buffer_store_b96(x, rsO, ov + ((j & 3) + 8 * (j >> 2)) * ROWB, 0, 2);
  }
}

extern "C" void kernel_launch(void* const* d_in, const int* in_sizes, int n_in, void* d_out, int out_size, void* d_ws, size_t ws_size,
                              hipStream_t stream) {
  lin_kernel<<<256, 768, 0, stream>>>((const char*)d_in[0], (const char*)d_in[1], (const char*)d_in[2], (const char*)d_in[3],
                                      (const char*)d_in[4], (char*)d_ws, (char*)d_out);
}
```

```cpp
#include <hip/hip_runtime.h>
#include <hip/hip_bf16.h>

typedef short s16x4 __attribute__((ext_vector_type(4)));
typedef __bf16 bf16x8 __attribute__((ext_vector_type(8)));
typedef __bf16 bf16x2 __attribute__((ext_vector_type(2)));
typedef float f32x2 __attribute__((ext_vector_type(2)));
typedef float f32x16 __attribute__((ext_vector_type(16)));
typedef unsigned u32x4 __attribute__((ext_vector_type(4)));
typedef unsigned u32x2 __attribute__((ext_vector_type(2)));
typedef unsigned u32x3 __attribute__((ext_vector_type(3)));
#define LAS __attribute__((address_space(3)))
#define GAS __attribute__((address_space(1)))
#define DI __device__ __forceinline__
#define RLX_AGENT __ATOMIC_RELAXED, __HIP_MEMORY_SCOPE_AGENT
#define RLX_WG __ATOMIC_RELAXED, __HIP_MEMORY_SCOPE_WORKGROUP
typedef GAS unsigned gu32;

#define CODEWARM 1
constexpr int CODE_BYTES = 15872;
constexpr int H = 32, SQ = 2048, SKV = 2048, DH = 192, R = 64;
constexpr int ROWB = DH * 2;
constexpr int SLOT = 16384;
constexpr int RING = 6, NST = 24;
constexpr int QSTART = 4;
constexpr int W1 = 24, W2 = 12, S2START = 6;
constexpr int LATEPUB = 20;
constexpr int POLLGAP = 3, RETRY_SLEEP = 32;
constexpr int PART_OFF = 49152;
constexpr int PAY_HEAD = 2 * 49152;
constexpr int CTL_OFF = H * PAY_HEAD;
constexpr int CTL_HEAD = 17 * 128;
constexpr unsigned POISON = 0xFFFFFFFFu;
constexpr unsigned SPIN_CAP = 1u << 20;
constexpr int VT = 4096 + 16;
constexpr int L_KP = 0, L_VP = 24576, L_SCR_HI = 49152 + 256, L_SCR_LO = L_SCR_HI + 49152, L_FLAG = L_SCR_LO + 49152, L_BYTES = L_FLAG + 64;
constexpr int F_ISS = 0, F_KP = 1, F_VP = 2, F_Q = 3, F_GO = 4  , F_Q2 = 6  ;

template <bool B> struct BoolC { static constexpr bool value = B; };
DI unsigned pk2(float a, float b) {
  f32x2 f = {a, b};
  return __builtin_bit_cast(unsigned, __builtin_convertvector(f, bf16x2));
}
DI f32x16 mfma32(u32x4 a, u32x4 b, f32x16 c) {
  return __builtin_amdgcn_mfma_f32_32x32x16_bf16(__builtin_bit_cast(bf16x8, a), __builtin_bit_cast(bf16x8, b), c, 0, 0, 0);
}
DI u32x4 tr2(LAS char* p0, LAS char* p1) {
  u32x2 l2 = __builtin_bit_cast(u32x2, __builtin_amdgcn_ds_read_tr16_b64_v4i16((LAS s16x4*)p0));
  u32x2 h2 = __builtin_bit_cast(u32x2, __builtin_amdgcn_ds_read_tr16_b64_v4i16((LAS s16x4*)p1));
  return u32x4{l2[0], l2[1], h2[0], h2[1]};
}
DI void dma_nt(unsigned lds, unsigned voff, __amdgpu_buffer_rsrc_t rs, unsigned soff) {
  unsigned keep;
  asm volatile("s_mov_b32 %0, m0\n\ts_mov_b32 m0, %1\n\ts_nop 0\n\tbuffer_load_dwordx4 %2, %3, %4 offen nt lds\n\ts_mov_b32 m0, %0"
               : "=&s"(keep) : "s"(lds), "v"(voff), "s"(rs), "s"(soff) : "memory");
}
DI void dma(unsigned lds, unsigned voff, __amdgpu_buffer_rsrc_t rs, unsigned soff) {
  unsigned keep;
  asm volatile("s_mov_b32 %0, m0\n\ts_mov_b32 m0, %1\n\ts_nop 0\n\tbuffer_load_dwordx4 %2, %3, %4 offen lds\n\ts_mov_b32 m0, %0"
               : "=&s"(keep) : "s"(lds), "v"(voff), "s"(rs), "s"(soff) : "memory");
}
template <int N> DI void vmcnt() { asm volatile("s_waitcnt vmcnt(%0)" ::"n"(N) : "memory"); }
DI void lgkm0() { asm volatile("s_waitcnt lgkmcnt(0)" ::: "memory"); }
DI void bar() {
  lgkm0();
  __builtin_amdgcn_s_barrier();
  asm volatile("" ::: "memory");
}
template <int N> DI void bars() {
#pragma unroll
  for (int i = 0; i < N; ++i) __builtin_amdgcn_s_barrier();
  asm volatile("" ::: "memory");
}
DI u32x4 ld_sc1(__amdgpu_buffer_rsrc_t rs, unsigned voff, unsigned soff) { return __builtin_amdgcn_raw_buffer_load_b128(rs, voff, soff, 16); }
DI void st_sc1(u32x4 v, __amdgpu_buffer_rsrc_t rs, unsigned voff) { __builtin_amdgcn_raw_buffer_store_b128(v, rs, voff, 0, 16); }
DI unsigned addbf2(unsigned x, unsigned y) {
  const float lo = __uint_as_float(x << 16) + __uint_as_float(y << 16);
  const float hi = __uint_as_float(x & 0xffff0000u) + __uint_as_float(y & 0xffff0000u);
  return pk2(lo, hi);
}
DI unsigned umin2(unsigned a, unsigned b) { return a < b ? a : b; }
DI unsigned umax2(unsigned a, unsigned b) { return a > b ? a : b; }
DI unsigned umax4(u32x4 v) { return umax2(umax2(v[0], v[1]), umax2(v[2], v[3])); }
DI u32x4 clean(u32x4 v) { return u32x4{umin2(v[0], POISON - 1u), umin2(v[1], POISON - 1u), umin2(v[2], POISON - 1u), umin2(v[3], POISON - 1u)}; }
DI void at_write(gu32* p, unsigned v) { asm volatile("global_atomic_swap %0, %1, off" ::"v"(p), "v"(v) : "memory"); }
DI void lds_wait(LAS unsigned* p, unsigned want) {
#pragma unroll 1
  for (unsigned spins = 0; spins < (SPIN_CAP << 6); ++spins) {
    if ((unsigned)__builtin_amdgcn_readfirstlane(__hip_atomic_load(p, RLX_WG)) >= want) break;
    __builtin_amdgcn_s_sleep(1);
  }
  asm volatile("" ::: "memory");
}

__global__ void __launch_bounds__(768, 3) lin_kernel(const char* __restrict__ q, const char* __restrict__ k, const char* __restrict__ v,
                                                     const char* __restrict__ e, const char* __restrict__ f, char* ws,
                                                     char* __restrict__ out) {
  __shared__ __attribute__((aligned(1024))) char smem[L_BYTES];
  LAS char* L = (LAS char*)smem;
  LAS unsigned* flag = (LAS unsigned*)(L + L_FLAG);
  unsigned long long pc0;
  asm volatile("s_getpc_b64 %0" : "=s"(pc0));
  __builtin_amdgcn_sched_barrier(0);
  const int b = blockIdx.x, xcd = b & 7, jj = b >> 3, head = xcd * 4 + (jj >> 3), sub = jj & 7;
  const int tensor = sub >> 2, role = sub & 3;
  const int tid = threadIdx.x, lane = tid & 63, w = __builtin_amdgcn_readfirstlane(tid >> 6);
  const bool helper = role == 3;
  const int lw = w & 3;
  const unsigned ldsb = (unsigned)(size_t)L;
  const __amdgpu_buffer_rsrc_t rsK =
      __builtin_amdgcn_make_buffer_rsrc((char*)(tensor ? v : k) + (size_t)head * SKV * ROWB, 0, SKV * ROWB, 0x00020000);
  const __amdgpu_buffer_rsrc_t rsE = __builtin_amdgcn_make_buffer_rsrc((char*)(tensor ? f : e), 0, R * SKV * 2, 0x00020000);
  unsigned sk = helper ? 1536u * ROWB : role * 128u, se = helper ? 3072u : 0u;
  int pairs = helper ? 16 : 0;
  unsigned voK[2], voE[2];
#pragma unroll
  for (int x = 0; x < 2; ++x) {
    const int row = 8 * (2 * lw + x) + (lane >> 3);
    voK[x] = row * ROWB + (((lane & 7) ^ (((lane >> 4) & 1) << 2)) << 4);
    voE[x] = row * (SKV * 2) + (((lane & 7) ^ ((x << 2) | (lane >> 4))) << 4);
  }
  unsigned dsl = ldsb + lw * 2048;
  auto issue = [&]() {
    dma_nt(dsl, voK[0], rsK, sk);
    dma_nt(dsl + 1024, voK[1], rsK, sk);
    dma(dsl + 8192, voE[0], rsE, se);
    dma(dsl + 9216, voE[1], rsE, se);
    if (pairs > 0) {
      --pairs;
      sk += pairs & 1 ? 128u : pairs ? 64u * ROWB - 128u : 128u - 448u * ROWB;
      se += pairs & 1 ? 0u : pairs ? 128u : -896u;
    } else {
      sk += 64u * ROWB;
      se += 128u;
    }
    dsl += SLOT;
    if (dsl >= ldsb + RING * SLOT) dsl -= RING * SLOT;
  };
  if ((w & 12) == 4) {
#pragma unroll
    for (int s = 0; s < RING - 1; ++s) issue();
  }
  __builtin_amdgcn_sched_barrier(0);
  const __amdgpu_buffer_rsrc_t rsP = __builtin_amdgcn_make_buffer_rsrc(ws + (size_t)head * PAY_HEAD, 0, PAY_HEAD, 0x00020000);
  gu32* ctl = (gu32*)(ws + CTL_OFF + head * CTL_HEAD);

  const __amdgpu_buffer_rsrc_t rsQ =
      __builtin_amdgcn_make_buffer_rsrc((char*)q + ((size_t)head * SQ + sub * 256) * ROWB, 0, 256 * ROWB, 0x00020000);
  unsigned voQ[6];
  auto qprep = [&]() {
#pragma unroll
    for (int bb = 0; bb < 3; ++bb) {
      const int idx = 64 * bb + lane, row = idx / 24, c = idx - row * 24;
      voQ[bb] = row * 384 + (((c & 24) | ((c & 7) ^ (row >> 1))) << 4);
      voQ[3 + bb] = (voQ[bb] ^ 64u) + 3072u;
    }
    asm volatile("" : "+v"(voQ[0]), "+v"(voQ[1]), "+v"(voQ[2]), "+v"(voQ[3]), "+v"(voQ[4]), "+v"(voQ[5]));
  };
  auto qgroup = [&](int j) {
    const unsigned d = ldsb + (j < 8 ? L_SCR_LO : L_SCR_HI - 49152) + j * 6144;
#pragma unroll
    for (int i = 0; i < 6; ++i) dma_nt(d + i * 1024, voQ[i], rsQ, j * 6144);
  };

  if (__builtin_expect(w == 8, 0)) {
    const int l8 = lane & 7;
    gu32* nonce_p = ctl + l8;
    gu32* inbox_p = ctl + (1 + 2 * sub + (l8 >> 2)) * 32 + (l8 & 3);
    gu32* note_p = ctl + (1 + 2 * l8 + tensor) * 32 + role;
    const unsigned old = __hip_atomic_load(lane < 8 ? inbox_p : ctl + sub, RLX_AGENT);
    if (lane < 8) flag[lane] = 0u;
    const u32x4 pz = {POISON, POISON, POISON, POISON};
#pragma unroll 1
    for (int s = helper ? 0 : 2; s < 3; ++s) {
      const unsigned base = lane * 16 + tensor * 24576 + (helper ? PART_OFF + s * 4096 : role * 4096);
#pragma unroll
      for (int i = 0; i < 8; ++i) st_sc1(pz, rsP, base + (i >> 2) * 12288 + (i & 3) * 1024);
    }
    lgkm0();
    bars<1>();
    unsigned n = __builtin_amdgcn_readlane(old, 8) + 1u;
    while (__builtin_amdgcn_ballot_w64(old == n) != 0ull) ++n;
    if (lane == 0) at_write(ctl + sub, n);
    bars<9>();
    unsigned echoed = __hip_atomic_load(nonce_p, RLX_AGENT);
    bars<6>();
    vmcnt<0>();
    asm volatile("" : "+v"(echoed));
    bars<8>();
    if (lane < 8) at_write(note_p, echoed);
    auto refresh = [&]() {
      const unsigned cur = __hip_atomic_load(nonce_p, RLX_AGENT);
      if (lane < 8 && cur != echoed) at_write(note_p, cur);
      echoed = cur;
    };
    bars<1>();
    unsigned seen = 0u;
#pragma unroll 1
    for (unsigned spins = 0; spins < (SPIN_CAP >> 3); ++spins) {
      if (lane < 8) {
        unsigned c0, c1, c2;
#define POLL_STEP(NEW, OLD)                                                                                                        \
  "global_atomic_add " NEW ", %4, %5, off sc0\n\ts_sleep %6\n\ts_waitcnt vmcnt(2)\n\tv_cmp_eq_u32 vcc, %9, " OLD "\n\t"           \
  "s_or_b32 %3, %3, vcc_lo\n\tv_and_b32 " OLD ", %3, %10\n\tv_cmp_eq_u32 vcc, " OLD ", %10\n\t"                                  \
  "s_and_saveexec_b64 s[6:7], vcc\n\tds_write_b32 %7, %8\n\ts_mov_b64 exec, s[6:7]\n\ts_cmp_eq_u32 %3, 0xff\n\ts_cbranch_scc1 2f\n\t"
        asm volatile(
            "global_atomic_add %0, %4, %5, off sc0\n\ts_sleep %6\n\t"
            "global_atomic_add %1, %4, %5, off sc0\n\ts_sleep %6\n\t"
            "s_mov_b32 s4, 7\n"
            "1:\n\t" POLL_STEP("%2", "%0") POLL_STEP("%0", "%1") POLL_STEP("%1", "%2")
            "s_sub_u32 s4, s4, 1\n\ts_cbranch_scc0 1b\n"
            "2:\n\t"
            "s_waitcnt vmcnt(0) lgkmcnt(0)"
            : "=&v"(c0), "=&v"(c1), "=&v"(c2), "+s"(seen)
            : "v"(inbox_p), "v"(0u), "n"(POLLGAP), "v"(ldsb + L_FLAG + (F_GO + (l8 >> 2)) * 4), "v"(1u), "s"(n), "v"(l8 & 4 ? 0xf0u : 0x0fu)
            : "memory", "vcc", "scc", "s4", "s6", "s7");
      }
      seen = __builtin_amdgcn_readfirstlane(seen);
      refresh();
      if (seen == 0xffu) break;
    }
    if (lane < 2) __hip_atomic_store(flag + F_GO + lane, 1u, RLX_WG);
    return;
  }
  if (__builtin_expect(w == 9, 0)) {
    qprep();
    bars<NST + 1 - QSTART>();
    int j = 0;
#pragma unroll 1
    for (int i = 0; i < QSTART; ++i) {
      qgroup(j++);
      bars<1>();
    }
#pragma unroll 1
    for (; j < 8; ++j) {
      if ((unsigned)__builtin_amdgcn_readfirstlane(__hip_atomic_load(flag + F_ISS, RLX_WG)) < 8u) {
        vmcnt<W1 - 6>();
        if (j + 1 - W1 / 6 > 0 && lane == 0) __hip_atomic_store(flag + F_Q, (unsigned)(j + 1 - W1 / 6), RLX_WG);
      }
      qgroup(j);
    }
#define QTAIL(F, N)                                                        \
  vmcnt<6 * (N)>();                                                        \
  if (lane == 0) __hip_atomic_store(flag + (F), 8u - (N), RLX_WG);
    vmcnt<36>();
    if (lane == 0) __hip_atomic_fetch_max(flag + F_Q, 2u, RLX_WG);
    vmcnt<24>();
    if (lane == 0) __hip_atomic_fetch_max(flag + F_Q, 4u, RLX_WG);
    QTAIL(F_Q, 3) QTAIL(F_Q, 2) QTAIL(F_Q, 1) QTAIL(F_Q, 0)
    return;
  }
  if (__builtin_expect(w >= 10, 0)) {
    if (CODEWARM) {
      const __amdgpu_buffer_rsrc_t rsC = __builtin_amdgcn_make_buffer_rsrc((char*)(pc0 & ~255ull), 0, CODE_BYTES, 0x00020000);
      if (CODEWARM == 1) dma(ldsb + L_SCR_LO + 4096 + (w - 10) * 1024, lane * 16, rsC, (sub * 2 + (w - 10)) * 1024);
      else {
#pragma unroll 1
        for (int i = 0; i < 8; ++i) dma(ldsb + L_SCR_LO + 4096 + (w - 10) * 1024, lane * 16, rsC, (i * 2 + (w - 10)) * 1024);
      }
    }
    qprep();
    bars<NST + 1>();
    bool wide = false;
#pragma unroll 1
    for (unsigned spins = 0; spins < (SPIN_CAP << 6); ++spins) {
      wide = (unsigned)__builtin_amdgcn_readfirstlane(__hip_atomic_load(flag + F_ISS, RLX_WG)) >= 8u;
      if (wide || (unsigned)__builtin_amdgcn_readfirstlane(__hip_atomic_load(flag + F_Q, RLX_WG)) >= S2START) break;
      __builtin_amdgcn_s_sleep(1);
    }
#pragma unroll 1
    for (int x = 0; x < 4; ++x) {
      if (!wide) {
        vmcnt<W2 - 6>();
        wide = (unsigned)__builtin_amdgcn_readfirstlane(__hip_atomic_load(flag + F_ISS, RLX_WG)) >= 8u;
      }
      qgroup(8 + 2 * (w - 10) + (x & 1) + 4 * (x >> 1));
    }
    vmcnt<12>();
    if (lane == 0) __hip_atomic_store(flag + F_Q2 + (w - 10), 1u, RLX_WG);
    vmcnt<0>();
    if (lane == 0) __hip_atomic_store(flag + F_Q2 + (w - 10), 2u, RLX_WG);
    return;
  }

  const int q4 = (lane & 15) >> 2, p4 = lane & 3, r32 = lane & 31, hh = lane >> 5;
  const int ch = w >> 2;
  const unsigned go = lw * 1024 + lane * 16, gs = ch * 24576;
  unsigned dst[6];
#pragma unroll
  for (int i = 0; i < 6; ++i) {
    const unsigned d = 64 * (i % 3) + 32 * (i / 3) + (r32 & 19) + ((r32 & 4) << 1) + ((r32 & 8) >> 1), d6 = d / 6u;
    dst[i] = ch ? L_VP + (d - 6 * d6) * VT + lw * 1024 + hh * 512 + d6 * 16 : L_KP + i * 4096 + go;
  }
  asm volatile("" : "+v"(dst[0]), "+v"(dst[1]), "+v"(dst[2]), "+v"(dst[3]), "+v"(dst[4]), "+v"(dst[5]));
  static_assert(RING >= 4 && RING <= 6, "ring depth");
  if (ch) {
#pragma unroll 1
    for (int s = 0; s < NST - (RING - 1); ++s) {
      vmcnt<4 * (RING - 2)>();
      bars<1>();
      issue();
    }
    if (RING > 5) { vmcnt<16>(); bars<1>(); }
    if (RING > 4) { vmcnt<12>(); bars<1>(); }
    vmcnt<8>(); bars<1>();
    vmcnt<4>(); bars<1>();
    vmcnt<0>(); bars<1>();
  } else {
    const int rq = lw & 1, cq = lw >> 1;
    const unsigned kt = (8 * hh + q4) * 128 + ((cq ^ (q4 >> 1)) << 6) + ((lane >> 4) & 1) * 32 + (p4 & 1) * 16 + (p4 >> 1) * 8;
    unsigned et[4];
#pragma unroll
    for (int kk = 0; kk < 4; ++kk) et[kk] = 8192 + (32 * rq + r32) * 128 + (((2 * kk + hh) ^ ((r32 >> 1) & 7)) << 4);
    const unsigned fin_v = tensor ? 24576 + cq * 12288 + rq * 2048 + lane * 16 : rq * 12288 + cq * 2048 + lane * 16;
    f32x16 acc, aco;
#pragma unroll
    for (int i = 0; i < 16; ++i) { acc[i] = 0.f; aco[i] = 0.f; }
    auto pack = [&](const f32x16& x, int t) {
      return clean(u32x4{pk2(x[8 * t], x[8 * t + 1]), pk2(x[8 * t + 2], x[8 * t + 3]), pk2(x[8 * t + 4], x[8 * t + 5]), pk2(x[8 * t + 6], x[8 * t + 7])});
    };
    auto run = [&](auto ISV) {
      u32x4 ea[4], eb[4], oa[4], ob[4];
#pragma unroll
      for (int kk = 0; kk < 4; ++kk) { oa[kk] = u32x4{0u, 0u, 0u, 0u}; ob[kk] = u32x4{0u, 0u, 0u, 0u}; }
      int so = 0;
      auto mul = [&](f32x16& c, u32x4(&fa_)[4], u32x4(&fb_)[4]) {
#pragma unroll
        for (int kk = 0; kk < 4; ++kk) c = ISV.value ? mfma32(fb_[kk], fa_[kk], c) : mfma32(fa_[kk], fb_[kk], c);
      };
      auto stage = [&](u32x4(&na)[4], u32x4(&nb)[4], f32x16& c, u32x4(&fa_)[4], u32x4(&fb_)[4]) {
        bars<1>();
        LAS char* sl = L + so;
#pragma unroll
        for (int kk = 0; kk < 4; ++kk) {
          na[kk] = tr2(sl + kt + kk * 2048, sl + kt + kk * 2048 + 512);
          nb[kk] = *(LAS u32x4*)(sl + et[kk]);
        }
        __builtin_amdgcn_sched_barrier(0);
        mul(c, fa_, fb_);
        __builtin_amdgcn_sched_barrier(0);
        lgkm0();
        so += SLOT;
        if (so == RING * SLOT) so = 0;
      };
      u32x4 held[4];
#pragma unroll
      for (int i = 0; i < 4; ++i) held[i] = u32x4{0u, 0u, 0u, 0u};
#pragma unroll 1
      for (int s = 0; s < NST; s += 2) {
        stage(ea, eb, aco, oa, ob);
        if (helper && s == 16) {
          held[0] = pack(acc, 0); held[1] = pack(acc, 1); held[2] = pack(aco, 0); held[3] = pack(aco, 1);
#pragma unroll
          for (int i = 0; i < 16; ++i) { acc[i] = 0.f; aco[i] = 0.f; }
        }
        if (helper && s == LATEPUB) {
#pragma unroll
          for (int i = 0; i < 4; ++i) st_sc1(held[i], rsP, fin_v + PART_OFF + (i >> 1) * 4096 + (i & 1) * 1024);
        }
        stage(oa, ob, acc, ea, eb);
      }
      mul(aco, oa, ob);
    };
    if (tensor) run(BoolC<true>{});
    else run(BoolC<false>{});
    acc += aco;
    const unsigned fin_o = fin_v + (helper ? PART_OFF + 8192 : role * 4096);
    st_sc1(pack(acc, 0), rsP, fin_o);
    st_sc1(pack(acc, 1), rsP, fin_o + 1024);
    asm volatile("" ::: "memory");
  }
  bar();
  LAS char* scr = L + (w < 4 ? L_SCR_LO : L_SCR_HI) + lw * 12288;
  unsigned fa[4];
#pragma unroll
  for (int k4 = 0; k4 < 4; ++k4) fa[k4] = r32 * 384 + (((2 * k4 + hh) ^ ((r32 >> 1) & 7)) << 4);
  LAS unsigned* qflag = flag + (ch ? F_Q2 + (lw & 1) : F_Q);
  const unsigned qwant = ch ? (lw >> 1) + 1 : 2 * lw + 2;
  lds_wait(flag + F_GO + ch, 1u);
  if (lane == 0) __hip_atomic_fetch_add(flag + F_ISS, 1u, RLX_WG);
#pragma unroll 1
  for (unsigned spins = 0; spins < SPIN_CAP; ++spins) {
    asm volatile("" ::: "memory");
    u32x4 gv[12];
#pragma unroll
    for (int i = 0; i < 12; ++i) gv[i] = ld_sc1(rsP, go, gs + (i >> 1) * 4096 + (i & 1) * PART_OFF);
    __builtin_amdgcn_sched_barrier(0);
    unsigned mx = 0u;
#pragma unroll
    for (int i = 0; i < 6; ++i) {
      const u32x4 x = gv[2 * i], y = gv[2 * i + 1];
      mx = umax2(mx, umax2(umax4(x), umax4(y)));
      *(LAS u32x4*)(L + dst[i]) = u32x4{addbf2(x[0], y[0]), addbf2(x[1], y[1]), addbf2(x[2], y[2]), addbf2(x[3], y[3])};
      __builtin_amdgcn_sched_barrier(0);
    }
    if (__builtin_amdgcn_ballot_w64(mx == POISON) == 0ull) break;
    __builtin_amdgcn_s_sleep(RETRY_SLEEP);
  }
  lgkm0();
  if (lane == 0) __hip_atomic_fetch_add(flag + F_KP + ch, 1u, RLX_WG);
  lds_wait(qflag, qwant);
  u32x4 qf[12];
#pragma unroll
  for (int ks = 0; ks < 12; ++ks) qf[ks] = *(LAS u32x4*)(scr + fa[ks & 3] + (ks >> 2) * 128);
  lds_wait(flag + F_KP, 4u);

  const float cs = 0.07216878364870322f * 1.4426950408889634f;
  auto xmax = [](float x) {
    auto rr = __builtin_amdgcn_permlane32_swap(__float_as_uint(x), __float_as_uint(x), false, false);
    return fmaxf(__uint_as_float(rr[0]), __uint_as_float(rr[1]));
  };
  f32x16 s0, s1;
#pragma unroll
  for (int i = 0; i < 16; ++i) { s0[i] = 0.f; s1[i] = 0.f; }
  float m0, sum0 = 0.f;
  {
    LAS u32x4* kf = (LAS u32x4*)(L + L_KP) + lane;
    u32x4 ka[24];
#pragma unroll
    for (int i = 0; i < 6; ++i) ka[i] = kf[i * 64];
    __builtin_amdgcn_sched_barrier(0);
#pragma unroll
    for (int i = 0; i < 12; ++i) {
      ka[i + 6] = kf[(i + 6) * 64];
      s0 = mfma32(ka[i], qf[i], s0);
      __builtin_amdgcn_sched_barrier(0);
    }
#pragma unroll
    for (int i = 12; i < 24; ++i) {
      if (i + 6 < 24) ka[i + 6] = kf[(i + 6) * 64];
      s1 = mfma32(ka[i], qf[i - 12], s1);
      if (i == 13) {
        m0 = s0[0];
#pragma unroll
        for (int j = 1; j < 8; ++j) m0 = fmaxf(m0, s0[j]);
      }
      if (i == 14) {
#pragma unroll
        for (int j = 8; j < 16; ++j) m0 = fmaxf(m0, s0[j]);
      }
      if (i == 15) m0 = xmax(m0) * cs;
      if (i >= 16) {
#pragma unroll
        for (int j = 2 * (i - 16); j < 2 * (i - 16) + 2; ++j) { s0[j] = __builtin_amdgcn_exp2f(__builtin_fmaf(s0[j], cs, -m0)); sum0 += s0[j]; }
      }
      __builtin_amdgcn_sched_barrier(0);
    }
  }
  float m = s1[0];
#pragma unroll
  for (int i = 1; i < 16; ++i) m = fmaxf(m, s1[i]);
  const float mc = fmaxf(xmax(m) * cs, m0);
  const float a0 = __builtin_amdgcn_exp2f(m0 - mc);
  float sum = sum0 * a0;
#pragma unroll
  for (int i = 0; i < 16; ++i) { s1[i] = __builtin_amdgcn_exp2f(__builtin_fmaf(s1[i], cs, -mc)); sum += s1[i]; }
  {
    auto rr = __builtin_amdgcn_permlane32_swap(__float_as_uint(sum), __float_as_uint(sum), false, false);
    sum = __uint_as_float(rr[0]) + __uint_as_float(rr[1]);
  }
  const float inv = __builtin_amdgcn_rcpf(sum), inv0 = inv * a0;
  u32x4 pf[4];
#pragma unroll
  for (int t = 0; t < 2; ++t) {
    pf[t] = u32x4{pk2(s0[8 * t] * inv0, s0[8 * t + 1] * inv0), pk2(s0[8 * t + 2] * inv0, s0[8 * t + 3] * inv0),
                  pk2(s0[8 * t + 4] * inv0, s0[8 * t + 5] * inv0), pk2(s0[8 * t + 6] * inv0, s0[8 * t + 7] * inv0)};
    pf[2 + t] = u32x4{pk2(s1[8 * t] * inv, s1[8 * t + 1] * inv), pk2(s1[8 * t + 2] * inv, s1[8 * t + 3] * inv),
                      pk2(s1[8 * t + 4] * inv, s1[8 * t + 5] * inv), pk2(s1[8 * t + 6] * inv, s1[8 * t + 7] * inv)};
  }
  lds_wait(flag + F_VP, 4u);
  f32x16 o[6];
#pragma unroll
  for (int t = 0; t < 6; ++t)
#pragma unroll
    for (int i = 0; i < 16; ++i) o[t][i] = 0.f;
  {
    LAS char* vb = L + L_VP + lane * 16;
    u32x4 va[24];
#pragma unroll
    for (int i = 0; i < 6; ++i) va[i] = *(LAS u32x4*)(vb + (i % 6) * VT + (i / 6) * 1024);
    __builtin_amdgcn_sched_barrier(0);
#pragma unroll
    for (int i = 0; i < 24; ++i) {
      if (i + 6 < 24) va[i + 6] = *(LAS u32x4*)(vb + ((i + 6) % 6) * VT + ((i + 6) / 6) * 1024);
      o[i % 6] = mfma32(pf[i / 6], va[i], o[i % 6]);
      __builtin_amdgcn_sched_barrier(0);
    }
  }
  const __amdgpu_buffer_rsrc_t rsO =
      __builtin_amdgcn_make_buffer_rsrc(out + ((size_t)head * SQ + sub * 256 + w * 32) * ROWB, 0, 32 * ROWB, 0x00020000);
  const unsigned ov = hh * 4 * ROWB + r32 * 12;
#pragma unroll
  for (int j = 0; j < 16; ++j) {
    const u32x3 x = {pk2(o[0][j], o[1][j]), pk2(o[2][j], o[3][j]), pk2(o[4][j], o[5][j])};
    __builtin_amdgcn_raw_buffer_store_b96(x, rsO, ov + ((j & 3) + 8 * (j >> 2)) * ROWB, 0, 2);
  }
}

extern "C" void kernel_launch(void* const* d_in, const int* in_sizes, int n_in, void* d_out, int out_size, void* d_ws, size_t ws_size,
                              hipStream_t stream) {
  lin_kernel<<<256, 768, 0, stream>>>((const char*)d_in[0], (const char*)d_in[1], (const char*)d_in[2], (const char*)d_in[3],
                                      (const char*)d_in[4], (char*)d_ws, (char*)d_out);
}
```

```cpp
#include <hip/hip_runtime.h>
#include <hip/hip_bf16.h>

typedef short s16x4 __attribute__((ext_vector_type(4)));
typedef __bf16 bf16x8 __attribute__((ext_vector_type(8)));
typedef __bf16 bf16x2 __attribute__((ext_vector_type(2)));
typedef float f32x2 __attribute__((ext_vector_type(2)));
typedef float f32x16 __attribute__((ext_vector_type(16)));
typedef unsigned u32x4 __attribute__((ext_vector_type(4)));
typedef unsigned u32x2 __attribute__((ext_vector_type(2)));
typedef unsigned u32x3 __attribute__((ext_vector_type(3)));
#define LAS __attribute__((address_space(3)))
#define GAS __attribute__((address_space(1)))
#define DI __device__ __forceinline__
#define RLX_AGENT __ATOMIC_RELAXED, __HIP_MEMORY_SCOPE_AGENT
#define RLX_WG __ATOMIC_RELAXED, __HIP_MEMORY_SCOPE_WORKGROUP
typedef GAS unsigned gu32;

#define CWW 2
constexpr int CODE_BYTES = 15872;
#define FIRST 2
constexpr int H = 32, SQ = 2048, SKV = 2048, DH = 192, R = 64;
constexpr int ROWB = DH * 2;
constexpr int SLOT = 16384;
constexpr int RING = 6, NST = 24;
constexpr int QSTART = 4;
constexpr int W1 = 24, W2 = 12, S2START = 6;
constexpr int LATEPUB = 20;
constexpr int POLLGAP = 3, RETRY_SLEEP = 32;
constexpr int PART_OFF = 49152;
constexpr int PAY_HEAD = 2 * 49152;
constexpr int CTL_OFF = H * PAY_HEAD;
constexpr int CTL_HEAD = 17 * 128;
constexpr unsigned POISON = 0xFFFFFFFFu;
constexpr unsigned SPIN_CAP = 1u << 20;
constexpr int VT = 4096 + 16;
constexpr int L_KP = 0, L_VP = 24576, L_SCR_HI = 49152 + 256, L_SCR_LO = L_SCR_HI + 49152, L_FLAG = L_SCR_LO + 49152, L_BYTES = L_FLAG + 64;
constexpr int F_ISS = 0, F_KP = 1, F_VP = 2, F_Q = 3, F_GO = 4  , F_Q2 = 6  ;

template <bool B> struct BoolC { static constexpr bool value = B; };
DI unsigned pk2(float a, float b) {
  f32x2 f = {a, b};
  return __builtin_bit_cast(unsigned, __builtin_convertvector(f, bf16x2));
}
DI f32x16 mfma32(u32x4 a, u32x4 b, f32x16 c) {
  return __builtin_amdgcn_mfma_f32_32x32x16_bf16(__builtin_bit_cast(bf16x8, a), __builtin_bit_cast(bf16x8, b), c, 0, 0, 0);
}
DI u32x4 tr2(LAS char* p0, LAS char* p1) {
  u32x2 l2 = __builtin_bit_cast(u32x2, __builtin_amdgcn_ds_read_tr16_b64_v4i16((LAS s16x4*)p0));
  u32x2 h2 = __builtin_bit_cast(u32x2, __builtin_amdgcn_ds_read_tr16_b64_v4i16((LAS s16x4*)p1));
  return u32x4{l2[0], l2[1], h2[0], h2[1]};
}
DI void dma_nt(unsigned lds, unsigned voff, __amdgpu_buffer_rsrc_t rs, unsigned soff) {
  unsigned keep;
  asm volatile("s_mov_b32 %0, m0\n\ts_mov_b32 m0, %1\n\ts_nop 0\n\tbuffer_load_dwordx4 %2, %3, %4 offen nt lds\n\ts_mov_b32 m0, %0"
               : "=&s"(keep) : "s"(lds), "v"(voff), "s"(rs), "s"(soff) : "memory");
}
DI void dma(unsigned lds, unsigned voff, __amdgpu_buffer_rsrc_t rs, unsigned soff) {
  unsigned keep;
  asm volatile("s_mov_b32 %0, m0\n\ts_mov_b32 m0, %1\n\ts_nop 0\n\tbuffer_load_dwordx4 %2, %3, %4 offen lds\n\ts_mov_b32 m0, %0"
               : "=&s"(keep) : "s"(lds), "v"(voff), "s"(rs), "s"(soff) : "memory");
}
template <int N> DI void vmcnt() { asm volatile("s_waitcnt vmcnt(%0)" ::"n"(N) : "memory"); }
DI void lgkm0() { asm volatile("s_waitcnt lgkmcnt(0)" ::: "memory"); }
DI void bar() {
  lgkm0();
  __builtin_amdgcn_s_barrier();
  asm volatile("" ::: "memory");
}
template <int N> DI void bars() {
#pragma unroll
  for (int i = 0; i < N; ++i) __builtin_amdgcn_s_barrier();
  asm volatile("" ::: "memory");
}
DI u32x4 ld_sc1(__amdgpu_buffer_rsrc_t rs, unsigned voff, unsigned soff) { return __builtin_amdgcn_raw_buffer_load_b128(rs, voff, soff, 16); }
DI void st_sc1(u32x4 v, __amdgpu_buffer_rsrc_t rs, unsigned voff) { __builtin_amdgcn_raw_buffer_store_b128(v, rs, voff, 0, 16); }
DI unsigned addbf2(unsigned x, unsigned y) {
  const float lo = __uint_as_float(x << 16) + __uint_as_float(y << 16);
  const float hi = __uint_as_float(x & 0xffff0000u) + __uint_as_float(y & 0xffff0000u);
  return pk2(lo, hi);
}
DI unsigned umin2(unsigned a, unsigned b) { return a < b ? a : b; }
DI unsigned umax2(unsigned a, unsigned b) { return a > b ? a : b; }
DI unsigned umax4(u32x4 v) { return umax2(umax2(v[0], v[1]), umax2(v[2], v[3])); }
DI u32x4 clean(u32x4 v) { return u32x4{umin2(v[0], POISON - 1u), umin2(v[1], POISON - 1u), umin2(v[2], POISON - 1u), umin2(v[3], POISON - 1u)}; }
DI void at_write(gu32* p, unsigned v) { asm volatile("global_atomic_swap %0, %1, off" ::"v"(p), "v"(v) : "memory"); }
DI void lds_wait(LAS unsigned* p, unsigned want) {
#pragma unroll 1
  for (unsigned spins = 0; spins < (SPIN_CAP << 6); ++spins) {
    if ((unsigned)__builtin_amdgcn_readfirstlane(__hip_atomic_load(p, RLX_WG)) >= want) break;
    __builtin_amdgcn_s_sleep(1);
  }
  asm volatile("" ::: "memory");
}

__global__ void __launch_bounds__(768, 3) lin_kernel(const char* __restrict__ q, const char* __restrict__ k, const char* __restrict__ v,
                                                     const char* __restrict__ e, const char* __restrict__ f, char* ws,
                                                     char* __restrict__ out) {
  __shared__ __attribute__((aligned(1024))) char smem[L_BYTES];
  LAS char* L = (LAS char*)smem;
  LAS unsigned* flag = (LAS unsigned*)(L + L_FLAG);
  unsigned long long pc0;
  asm volatile("s_getpc_b64 %0" : "=s"(pc0));
  __builtin_amdgcn_sched_barrier(0);
  const int b = blockIdx.x, xcd = b & 7, jj = b >> 3, head = xcd * 4 + (jj >> 3), sub = jj & 7;
  const int tensor = sub >> 2, role = sub & 3;
  const int tid = threadIdx.x, lane = tid & 63, w = __builtin_amdgcn_readfirstlane(tid >> 6);
  const bool helper = role == 3;
  const int lw = w & 3;
  const unsigned ldsb = (unsigned)(size_t)L;
  if (w < CWW) {
    const __amdgpu_buffer_rsrc_t rsC = __builtin_amdgcn_make_buffer_rsrc((char*)(pc0 & ~255ull), 0, CODE_BYTES, 0x00020000);
    asm volatile("s_nop 4");
    dma(ldsb + L_SCR_LO + 4096 + w * 1024, lane * 16, rsC, ((sub * CWW + w) & 15) * 1024);
  }
  __builtin_amdgcn_sched_barrier(0);
  const __amdgpu_buffer_rsrc_t rsK =
      __builtin_amdgcn_make_buffer_rsrc((char*)(tensor ? v : k) + (size_t)head * SKV * ROWB, 0, SKV * ROWB, 0x00020000);
  const __amdgpu_buffer_rsrc_t rsE = __builtin_amdgcn_make_buffer_rsrc((char*)(tensor ? f : e), 0, R * SKV * 2, 0x00020000);
  unsigned sk = helper ? 1536u * ROWB : role * 128u, se = helper ? 3072u : 0u;
  int pairs = helper ? 16 : 0;
  unsigned voK[2], voE[2];
#pragma unroll
  for (int x = 0; x < 2; ++x) {
    const int row = 8 * (2 * lw + x) + (lane >> 3);
    voK[x] = row * ROWB + (((lane & 7) ^ (((lane >> 4) & 1) << 2)) << 4);
    voE[x] = row * (SKV * 2) + (((lane & 7) ^ ((x << 2) | (lane >> 4))) << 4);
  }
  unsigned dsl = ldsb + lw * 2048;
  auto issue = [&]() {
    dma_nt(dsl, voK[0], rsK, sk);
    dma_nt(dsl + 1024, voK[1], rsK, sk);
    dma(dsl + 8192, voE[0], rsE, se);
    dma(dsl + 9216, voE[1], rsE, se);
    if (pairs > 0) {
      --pairs;
      sk += pairs & 1 ? 128u : pairs ? 64u * ROWB - 128u : 128u - 448u * ROWB;
      se += pairs & 1 ? 0u : pairs ? 128u : -896u;
    } else {
      sk += 64u * ROWB;
      se += 128u;
    }
    dsl += SLOT;
    if (dsl >= ldsb + RING * SLOT) dsl -= RING * SLOT;
  };
  if ((w & 12) == 4) {
    if (FIRST && (role == 1 || role == 2)) {
      const int hq = jj >> 3;
      const size_t hoff = (size_t)head * SQ * ROWB + (hq == 3 ? SQ * ROWB - 16 : 0);
      const char* tb = tensor ? (role == 1 ? q : out) + hoff
                              : role == 1 ? (hq & 1 ? f : e) : ws + (hq & 1 ? CTL_OFF + head * CTL_HEAD : head * PAY_HEAD);
      if (FIRST == 1 || !tensor) dma_nt(ldsb + L_SCR_LO + 8192 + lw * 1024, 0u, __builtin_amdgcn_make_buffer_rsrc((char*)tb, 0, 16, 0x00020000), 0);
    }
#pragma unroll
    for (int s = 0; s < RING - 1; ++s) issue();
  }
  __builtin_amdgcn_sched_barrier(0);
  const __amdgpu_buffer_rsrc_t rsP = __builtin_amdgcn_make_buffer_rsrc(ws + (size_t)head * PAY_HEAD, 0, PAY_HEAD, 0x00020000);
  gu32* ctl = (gu32*)(ws + CTL_OFF + head * CTL_HEAD);

  const __amdgpu_buffer_rsrc_t rsQ =
      __builtin_amdgcn_make_buffer_rsrc((char*)q + ((size_t)head * SQ + sub * 256) * ROWB, 0, 256 * ROWB, 0x00020000);
  unsigned voQ[6];
  auto qprep = [&]() {
#pragma unroll
    for (int bb = 0; bb < 3; ++bb) {
      const int idx = 64 * bb + lane, row = idx / 24, c = idx - row * 24;
      voQ[bb] = row * 384 + (((c & 24) | ((c & 7) ^ (row >> 1))) << 4);
      voQ[3 + bb] = (voQ[bb] ^ 64u) + 3072u;
    }
    asm volatile("" : "+v"(voQ[0]), "+v"(voQ[1]), "+v"(voQ[2]), "+v"(voQ[3]), "+v"(voQ[4]), "+v"(voQ[5]));
  };
  auto qgroup = [&](int j) {
    const unsigned d = ldsb + (j < 8 ? L_SCR_LO : L_SCR_HI - 49152) + j * 6144;
#pragma unroll
    for (int i = 0; i < 6; ++i) dma_nt(d + i * 1024, voQ[i], rsQ, j * 6144);
  };

  if (__builtin_expect(w == 8, 0)) {
    const int l8 = lane & 7;
    gu32* nonce_p = ctl + l8;
    gu32* inbox_p = ctl + (1 + 2 * sub + (l8 >> 2)) * 32 + (l8 & 3);
    gu32* note_p = ctl + (1 + 2 * l8 + tensor) * 32 + role;
    const unsigned old = __hip_atomic_load(lane < 8 ? inbox_p : ctl + sub, RLX_AGENT);
    if (lane < 8) flag[lane] = 0u;
    const u32x4 pz = {POISON, POISON, POISON, POISON};
#pragma unroll 1
    for (int s = helper ? 0 : 2; s < 3; ++s) {
      const unsigned base = lane * 16 + tensor * 24576 + (helper ? PART_OFF + s * 4096 : role * 4096);
#pragma unroll
      for (int i = 0; i < 8; ++i) st_sc1(pz, rsP, base + (i >> 2) * 12288 + (i & 3) * 1024);
    }
    lgkm0();
    bars<1>();
    unsigned n = __builtin_amdgcn_readlane(old, 8) + 1u;
    while (__builtin_amdgcn_ballot_w64(old == n) != 0ull) ++n;
    if (lane == 0) at_write(ctl + sub, n);
    bars<9>();
    unsigned echoed = __hip_atomic_load(nonce_p, RLX_AGENT);
    bars<6>();
    vmcnt<0>();
    asm volatile("" : "+v"(echoed));
    bars<8>();
    if (lane < 8) at_write(note_p, echoed);
    auto refresh = [&]() {
      const unsigned cur = __hip_atomic_load(nonce_p, RLX_AGENT);
      if (lane < 8 && cur != echoed) at_write(note_p, cur);
      echoed = cur;
    };
    bars<1>();
    unsigned seen = 0u;
#pragma unroll 1
    for (unsigned spins = 0; spins < (SPIN_CAP >> 3); ++spins) {
      if (lane < 8) {
        unsigned c0, c1, c2;
#define POLL_STEP(NEW, OLD)                                                                                                        \
  "global_atomic_add " NEW ", %4, %5, off sc0\n\ts_sleep %6\n\ts_waitcnt vmcnt(2)\n\tv_cmp_eq_u32 vcc, %9, " OLD "\n\t"           \
  "s_or_b32 %3, %3, vcc_lo\n\tv_and_b32 " OLD ", %3, %10\n\tv_cmp_eq_u32 vcc, " OLD ", %10\n\t"                                  \
  "s_and_saveexec_b64 s[6:7], vcc\n\tds_write_b32 %7, %8\n\ts_mov_b64 exec, s[6:7]\n\ts_cmp_eq_u32 %3, 0xff\n\ts_cbranch_scc1 2f\n\t"
        asm volatile(
            "global_atomic_add %0, %4, %5, off sc0\n\ts_sleep %6\n\t"
            "global_atomic_add %1, %4, %5, off sc0\n\ts_sleep %6\n\t"
            "s_mov_b32 s4, 7\n"
            "1:\n\t" POLL_STEP("%2", "%0") POLL_STEP("%0", "%1") POLL_STEP("%1", "%2")
            "s_sub_u32 s4, s4, 1\n\ts_cbranch_scc0 1b\n"
            "2:\n\t"
            "s_waitcnt vmcnt(0) lgkmcnt(0)"
            : "=&v"(c0), "=&v"(c1), "=&v"(c2), "+s"(seen)
            : "v"(inbox_p), "v"(0u), "n"(POLLGAP), "v"(ldsb + L_FLAG + (F_GO + (l8 >> 2)) * 4), "v"(1u), "s"(n), "v"(l8 & 4 ? 0xf0u : 0x0fu)
            : "memory", "vcc", "scc", "s4", "s6", "s7");
      }
      seen = __builtin_amdgcn_readfirstlane(seen);
      refresh();
      if (seen == 0xffu) break;
    }
    if (lane < 2) __hip_atomic_store(flag + F_GO + lane, 1u, RLX_WG);
    return;
  }
  if (__builtin_expect(w == 9, 0)) {
    qprep();
    bars<NST + 1 - QSTART>();
    int j = 0;
#pragma unroll 1
    for (int i = 0; i < QSTART; ++i) {
      qgroup(j++);
      bars<1>();
    }
#pragma unroll 1
    for (; j < 8; ++j) {
      if ((unsigned)__builtin_amdgcn_readfirstlane(__hip_atomic_load(flag + F_ISS, RLX_WG)) < 8u) {
        vmcnt<W1 - 6>();
        if (j + 1 - W1 / 6 > 0 && lane == 0) __hip_atomic_store(flag + F_Q, (unsigned)(j + 1 - W1 / 6), RLX_WG);
      }
      qgroup(j);
    }
#define QTAIL(F, N)                                                        \
  vmcnt<6 * (N)>();                                                        \
  if (lane == 0) __hip_atomic_store(flag + (F), 8u - (N), RLX_WG);
    vmcnt<36>();
    if (lane == 0) __hip_atomic_fetch_max(flag + F_Q, 2u, RLX_WG);
    vmcnt<24>();
    if (lane == 0) __hip_atomic_fetch_max(flag + F_Q, 4u, RLX_WG);
    QTAIL(F_Q, 3) QTAIL(F_Q, 2) QTAIL(F_Q, 1) QTAIL(F_Q, 0)
    return;
  }
  if (__builtin_expect(w >= 10, 0)) {
    qprep();
    bars<NST + 1>();
    bool wide = false;
#pragma unroll 1
    for (unsigned spins = 0; spins < (SPIN_CAP << 6); ++spins) {
      wide = (unsigned)__builtin_amdgcn_readfirstlane(__hip_atomic_load(flag + F_ISS, RLX_WG)) >= 8u;
      if (wide || (unsigned)__builtin_amdgcn_readfirstlane(__hip_atomic_load(flag + F_Q, RLX_WG)) >= S2START) break;
      __builtin_amdgcn_s_sleep(1);
    }
#pragma unroll 1
    for (int x = 0; x < 4; ++x) {
      if (!wide) {
        vmcnt<W2 - 6>();
        wide = (unsigned)__builtin_amdgcn_readfirstlane(__hip_atomic_load(flag + F_ISS, RLX_WG)) >= 8u;
      }
      qgroup(8 + 2 * (w - 10) + (x & 1) + 4 * (x >> 1));
    }
    vmcnt<12>();
    if (lane == 0) __hip_atomic_store(flag + F_Q2 + (w - 10), 1u, RLX_WG);
    vmcnt<0>();
    if (lane == 0) __hip_atomic_store(flag + F_Q2 + (w - 10), 2u, RLX_WG);
    return;
  }

  const int q4 = (lane & 15) >> 2, p4 = lane & 3, r32 = lane & 31, hh = lane >> 5;
  const int ch = w >> 2;
  const unsigned go = lw * 1024 + lane * 16, gs = ch * 24576;
  unsigned dst[6];
#pragma unroll
  for (int i = 0; i < 6; ++i) {
    const unsigned d = 64 * (i % 3) + 32 * (i / 3) + (r32 & 19) + ((r32 & 4) << 1) + ((r32 & 8) >> 1), d6 = d / 6u;
    dst[i] = ch ? L_VP + (d - 6 * d6) * VT + lw * 1024 + hh * 512 + d6 * 16 : L_KP + i * 4096 + go;
  }
  asm volatile("" : "+v"(dst[0]), "+v"(dst[1]), "+v"(dst[2]), "+v"(dst[3]), "+v"(dst[4]), "+v"(dst[5]));
  static_assert(RING >= 4 && RING <= 6, "ring depth");
  if (ch) {
#pragma unroll 1
    for (int s = 0; s < NST - (RING - 1); ++s) {
      vmcnt<4 * (RING - 2)>();
      bars<1>();
      issue();
    }
    if (RING > 5) { vmcnt<16>(); bars<1>(); }
    if (RING > 4) { vmcnt<12>(); bars<1>(); }
    vmcnt<8>(); bars<1>();
    vmcnt<4>(); bars<1>();
    vmcnt<0>(); bars<1>();
  } else {
    const int rq = lw & 1, cq = lw >> 1;
    const unsigned kt = (8 * hh + q4) * 128 + ((cq ^ (q4 >> 1)) << 6) + ((lane >> 4) & 1) * 32 + (p4 & 1) * 16 + (p4 >> 1) * 8;
    unsigned et[4];
#pragma unroll
    for (int kk = 0; kk < 4; ++kk) et[kk] = 8192 + (32 * rq + r32) * 128 + (((2 * kk + hh) ^ ((r32 >> 1) & 7)) << 4);
    const unsigned fin_v = tensor ? 24576 + cq * 12288 + rq * 2048 + lane * 16 : rq * 12288 + cq * 2048 + lane * 16;
    f32x16 acc, aco;
#pragma unroll
    for (int i = 0; i < 16; ++i) { acc[i] = 0.f; aco[i] = 0.f; }
    auto pack = [&](const f32x16& x, int t) {
      return clean(u32x4{pk2(x[8 * t], x[8 * t + 1]), pk2(x[8 * t + 2], x[8 * t + 3]), pk2(x[8 * t + 4], x[8 * t + 5]), pk2(x[8 * t + 6], x[8 * t + 7])});
    };
    auto run = [&](auto ISV) {
      u32x4 ea[4], eb[4], oa[4], ob[4];
#pragma unroll
      for (int kk = 0; kk < 4; ++kk) { oa[kk] = u32x4{0u, 0u, 0u, 0u}; ob[kk] = u32x4{0u, 0u, 0u, 0u}; }
      int so = 0;
      auto mul = [&](f32x16& c, u32x4(&fa_)[4], u32x4(&fb_)[4]) {
#pragma unroll
        for (int kk = 0; kk < 4; ++kk) c = ISV.value ? mfma32(fb_[kk], fa_[kk], c) : mfma32(fa_[kk], fb_[kk], c);
      };
      auto stage = [&](u32x4(&na)[4], u32x4(&nb)[4], f32x16& c, u32x4(&fa_)[4], u32x4(&fb_)[4]) {
        bars<1>();
        LAS char* sl = L + so;
#pragma unroll
        for (int kk = 0; kk < 4; ++kk) {
          na[kk] = tr2(sl + kt + kk * 2048, sl + kt + kk * 2048 + 512);
          nb[kk] = *(LAS u32x4*)(sl + et[kk]);
        }
        __builtin_amdgcn_sched_barrier(0);
        mul(c, fa_, fb_);
        __builtin_amdgcn_sched_barrier(0);
        lgkm0();
        so += SLOT;
        if (so == RING * SLOT) so = 0;
      };
      u32x4 held[4];
#pragma unroll
      for (int i = 0; i < 4; ++i) held[i] = u32x4{0u, 0u, 0u, 0u};
#pragma unroll 1
      for (int s = 0; s < NST; s += 2) {
        stage(ea, eb, aco, oa, ob);
        if (helper && s == 16) {
          held[0] = pack(acc, 0); held[1] = pack(acc, 1); held[2] = pack(aco, 0); held[3] = pack(aco, 1);
#pragma unroll
          for (int i = 0; i < 16; ++i) { acc[i] = 0.f; aco[i] = 0.f; }
        }
        if (helper && s == LATEPUB) {
#pragma unroll
          for (int i = 0; i < 4; ++i) st_sc1(held[i], rsP, fin_v + PART_OFF + (i >> 1) * 4096 + (i & 1) * 1024);
        }
        stage(oa, ob, acc, ea, eb);
      }
      mul(aco, oa, ob);
    };
    if (tensor) run(BoolC<true>{});
    else run(BoolC<false>{});
    acc += aco;
    const unsigned fin_o = fin_v + (helper ? PART_OFF + 8192 : role * 4096);
    st_sc1(pack(acc, 0), rsP, fin_o);
    st_sc1(pack(acc, 1), rsP, fin_o + 1024);
    asm volatile("" ::: "memory");
  }
  bar();
  LAS char* scr = L + (w < 4 ? L_SCR_LO : L_SCR_HI) + lw * 12288;
  unsigned fa[4];
#pragma unroll
  for (int k4 = 0; k4 < 4; ++k4) fa[k4] = r32 * 384 + (((2 * k4 + hh) ^ ((r32 >> 1) & 7)) << 4);
  LAS unsigned* qflag = flag + (ch ? F_Q2 + (lw & 1) : F_Q);
  const unsigned qwant = ch ? (lw >> 1) + 1 : 2 * lw + 2;
  lds_wait(flag + F_GO + ch, 1u);
  if (lane == 0) __hip_atomic_fetch_add(flag + F_ISS, 1u, RLX_WG);
#pragma unroll 1
  for (unsigned spins = 0; spins < SPIN_CAP; ++spins) {
    asm volatile("" ::: "memory");
    u32x4 gv[12];
#pragma unroll
    for (int i = 0; i < 12; ++i) gv[i] = ld_sc1(rsP, go, gs + (i >> 1) * 4096 + (i & 1) * PART_OFF);
    __builtin_amdgcn_sched_barrier(0);
    unsigned mx = 0u;
#pragma unroll
    for (int i = 0; i < 6; ++i) {
      const u32x4 x = gv[2 * i], y = gv[2 * i + 1];
      mx = umax2(mx, umax2(umax4(x), umax4(y)));
      *(LAS u32x4*)(L + dst[i]) = u32x4{addbf2(x[0], y[0]), addbf2(x[1], y[1]), addbf2(x[2], y[2]), addbf2(x[3], y[3])};
      __builtin_amdgcn_sched_barrier(0);
    }
    if (__builtin_amdgcn_ballot_w64(mx == POISON) == 0ull) break;
    __builtin_amdgcn_s_sleep(RETRY_SLEEP);
  }
  lgkm0();
  if (lane == 0) __hip_atomic_fetch_add(flag + F_KP + ch, 1u, RLX_WG);
  lds_wait(qflag, qwant);
  u32x4 qf[12];
#pragma unroll
  for (int ks = 0; ks < 12; ++ks) qf[ks] = *(LAS u32x4*)(scr + fa[ks & 3] + (ks >> 2) * 128);
  lds_wait(flag + F_KP, 4u);

  const float cs = 0.07216878364870322f * 1.4426950408889634f;
  auto xmax = [](float x) {
    auto rr = __builtin_amdgcn_permlane32_swap(__float_as_uint(x), __float_as_uint(x), false, false);
    return fmaxf(__uint_as_float(rr[0]), __uint_as_float(rr[1]));
  };
  f32x16 s0, s1;
#pragma unroll
  for (int i = 0; i < 16; ++i) { s0[i] = 0.f; s1[i] = 0.f; }
  float m0, sum0 = 0.f;
  {
    LAS u32x4* kf = (LAS u32x4*)(L + L_KP) + lane;
    u32x4 ka[24];
#pragma unroll
    for (int i = 0; i < 6; ++i) ka[i] = kf[i * 64];
    __builtin_amdgcn_sched_barrier(0);
#pragma unroll
    for (int i = 0; i < 12; ++i) {
      ka[i + 6] = kf[(i + 6) * 64];
      s0 = mfma32(ka[i], qf[i], s0);
      __builtin_amdgcn_sched_barrier(0);
    }
#pragma unroll
    for (int i = 12; i < 24; ++i) {
      if (i + 6 < 24) ka[i + 6] = kf[(i + 6) * 64];
      s1 = mfma32(ka[i], qf[i - 12], s1);
      if (i == 13) {
        m0 = s0[0];
#pragma unroll
        for (int j = 1; j < 8; ++j) m0 = fmaxf(m0, s0[j]);
      }
      if (i == 14) {
#pragma unroll
        for (int j = 8; j < 16; ++j) m0 = fmaxf(m0, s0[j]);
      }
      if (i == 15) m0 = xmax(m0) * cs;
      if (i >= 16) {
#pragma unroll
        for (int j = 2 * (i - 16); j < 2 * (i - 16) + 2; ++j) { s0[j] = __builtin_amdgcn_exp2f(__builtin_fmaf(s0[j], cs, -m0)); sum0 += s0[j]; }
      }
      __builtin_amdgcn_sched_barrier(0);
    }
  }
  float m = s1[0];
#pragma unroll
  for (int i = 1; i < 16; ++i) m = fmaxf(m, s1[i]);
  const float mc = fmaxf(xmax(m) * cs, m0);
  const float a0 = __builtin_amdgcn_exp2f(m0 - mc);
  float sum = sum0 * a0;
#pragma unroll
  for (int i = 0; i < 16; ++i) { s1[i] = __builtin_amdgcn_exp2f(__builtin_fmaf(s1[i], cs, -mc)); sum += s1[i]; }
  {
    auto rr = __builtin_amdgcn_permlane32_swap(__float_as_uint(sum), __float_as_uint(sum), false, false);
    sum = __uint_as_float(rr[0]) + __uint_as_float(rr[1]);
  }
  const float inv = __builtin_amdgcn_rcpf(sum), inv0 = inv * a0;
  u32x4 pf[4];
#pragma unroll
  for (int t = 0; t < 2; ++t) {
    pf[t] = u32x4{pk2(s0[8 * t] * inv0, s0[8 * t + 1] * inv0), pk2(s0[8 * t + 2] * inv0, s0[8 * t + 3] * inv0),
                  pk2(s0[8 * t + 4] * inv0, s0[8 * t + 5] * inv0), pk2(s0[8 * t + 6] * inv0, s0[8 * t + 7] * inv0)};
    pf[2 + t] = u32x4{pk2(s1[8 * t] * inv, s1[8 * t + 1] * inv), pk2(s1[8 * t + 2] * inv, s1[8 * t + 3] * inv),
                      pk2(s1[8 * t + 4] * inv, s1[8 * t + 5] * inv), pk2(s1[8 * t + 6] * inv, s1[8 * t + 7] * inv)};
  }
  lds_wait(flag + F_VP, 4u);
  f32x16 o[6];
#pragma unroll
  for (int t = 0; t < 6; ++t)
#pragma unroll
    for (int i = 0; i < 16; ++i) o[t][i] = 0.f;
  {
    LAS char* vb = L + L_VP + lane * 16;
    u32x4 va[24];
#pragma unroll
    for (int i = 0; i < 6; ++i) va[i] = *(LAS u32x4*)(vb + (i % 6) * VT + (i / 6) * 1024);
    __builtin_amdgcn_sched_barrier(0);
#pragma unroll
    for (int i = 0; i < 24; ++i) {
      if (i + 6 < 24) va[i + 6] = *(LAS u32x4*)(vb + ((i + 6) % 6) * VT + ((i + 6) / 6) * 1024);
      o[i % 6] = mfma32(pf[i / 6], va[i], o[i % 6]);
      __builtin_amdgcn_sched_barrier(0);
    }
  }
  const __amdgpu_buffer_rsrc_t rsO =
      __builtin_amdgcn_make_buffer_rsrc(out + ((size_t)head * SQ + sub * 256 + w * 32) * ROWB, 0, 32 * ROWB, 0x00020000);
  const unsigned ov = hh * 4 * ROWB + r32 * 12;
#pragma unroll
  for (int j = 0; j < 16; ++j) {
    const u32x3 x = {pk2(o[0][j], o[1][j]), pk2(o[2][j], o[3][j]), pk2(o[4][j], o[5][j])};
    __builtin_amdgcn_raw_buffer_store_b96(x, rsO, ov + ((j & 3) + 8 * (j >> 2)) * ROWB, 0, 2);
  }
}

extern "C" void kernel_launch(void* const* d_in, const int* in_sizes, int n_in, void* d_out, int out_size, void* d_ws, size_t ws_size,
                              hipStream_t stream) {
  lin_kernel<<<256, 768, 0, stream>>>((const char*)d_in[0], (const char*)d_in[1], (const char*)d_in[2], (const char*)d_in[3],
                                      (const char*)d_in[4], (char*)d_ws, (char*)d_out);
}
```

```cpp
#include <hip/hip_runtime.h>
#include <hip/hip_bf16.h>

typedef short s16x4 __attribute__((ext_vector_type(4)));
typedef __bf16 bf16x8 __attribute__((ext_vector_type(8)));
typedef __bf16 bf16x2 __attribute__((ext_vector_type(2)));
typedef float f32x2 __attribute__((ext_vector_type(2)));
typedef float f32x16 __attribute__((ext_vector_type(16)));
typedef unsigned u32x4 __attribute__((ext_vector_type(4)));
typedef unsigned u32x2 __attribute__((ext_vector_type(2)));
typedef unsigned u32x3 __attribute__((ext_vector_type(3)));
#define LAS __attribute__((address_space(3)))
#define GAS __attribute__((address_space(1)))
#define DI __device__ __forceinline__
#define RLX_AGENT __ATOMIC_RELAXED, __HIP_MEMORY_SCOPE_AGENT
#define RLX_WG __ATOMIC_RELAXED, __HIP_MEMORY_SCOPE_WORKGROUP
typedef GAS unsigned gu32;

constexpr int CODE_BYTES = 15872;
constexpr int H = 32, SQ = 2048, SKV = 2048, DH = 192, R = 64;
constexpr int ROWB = DH * 2;
constexpr int SLOT = 16384;
constexpr int RING = 6, NST = 24;
constexpr int QSTART = 4;
constexpr int W1 = 24, W2 = 12, S2START = 6;
constexpr int LATEPUB = 20;
constexpr int POLLGAP = 3, RETRY_SLEEP = 32;
constexpr int PART_OFF = 49152;
constexpr int PAY_HEAD = 2 * 49152;
constexpr int WS_HEAD = PAY_HEAD + 4096;
constexpr unsigned POISON = 0xFFFFFFFFu;
constexpr unsigned SPIN_CAP = 1u << 20;
constexpr int VT = 4096 + 16;
constexpr int L_KP = 0, L_VP = 24576, L_SCR_HI = 49152 + 256, L_SCR_LO = L_SCR_HI + 49152, L_FLAG = L_SCR_LO + 49152;
constexpr int L_DUMP = L_FLAG + 64, L_BYTES = L_DUMP + 1024;
constexpr int F_ISS = 0, F_KP = 1, F_VP = 2, F_Q = 3, F_GO = 4  , F_Q2 = 6  ;

template <bool B> struct BoolC { static constexpr bool value = B; };
DI unsigned pk2(float a, float b) {
  f32x2 f = {a, b};
  return __builtin_bit_cast(unsigned, __builtin_convertvector(f, bf16x2));
}
DI f32x16 mfma32(u32x4 a, u32x4 b, f32x16 c) {
  return __builtin_amdgcn_mfma_f32_32x32x16_bf16(__builtin_bit_cast(bf16x8, a), __builtin_bit_cast(bf16x8, b), c, 0, 0, 0);
}
DI u32x4 tr2(LAS char* p0, LAS char* p1) {
  u32x2 l2 = __builtin_bit_cast(u32x2, __builtin_amdgcn_ds_read_tr16_b64_v4i16((LAS s16x4*)p0));
  u32x2 h2 = __builtin_bit_cast(u32x2, __builtin_amdgcn_ds_read_tr16_b64_v4i16((LAS s16x4*)p1));
  return u32x4{l2[0], l2[1], h2[0], h2[1]};
}
DI void dma_nt(unsigned lds, unsigned voff, __amdgpu_buffer_rsrc_t rs, unsigned soff) {
  unsigned keep;
  asm volatile("s_mov_b32 %0, m0\n\ts_mov_b32 m0, %1\n\ts_nop 0\n\tbuffer_load_dwordx4 %2, %3, %4 offen nt lds\n\ts_mov_b32 m0, %0"
               : "=&s"(keep) : "s"(lds), "v"(voff), "s"(rs), "s"(soff) : "memory");
}
DI void dma(unsigned lds, unsigned voff, __amdgpu_buffer_rsrc_t rs, unsigned soff) {
  unsigned keep;
  asm volatile("s_mov_b32 %0, m0\n\ts_mov_b32 m0, %1\n\ts_nop 0\n\tbuffer_load_dwordx4 %2, %3, %4 offen lds\n\ts_mov_b32 m0, %0"
               : "=&s"(keep) : "s"(lds), "v"(voff), "s"(rs), "s"(soff) : "memory");
}
template <int N> DI void vmcnt() { asm volatile("s_waitcnt vmcnt(%0)" ::"n"(N) : "memory"); }
DI void lgkm0() { asm volatile("s_waitcnt lgkmcnt(0)" ::: "memory"); }
DI void bar() {
  lgkm0();
  __builtin_amdgcn_s_barrier();
  asm volatile("" ::: "memory");
}
template <int N> DI void bars() {
#pragma unroll
  for (int i = 0; i < N; ++i) __builtin_amdgcn_s_barrier();
  asm volatile("" ::: "memory");
}
DI u32x4 ld_sc1(__amdgpu_buffer_rsrc_t rs, unsigned voff, unsigned soff) { return __builtin_amdgcn_raw_buffer_load_b128(rs, voff, soff, 16); }
DI void st_sc1(u32x4 v, __amdgpu_buffer_rsrc_t rs, unsigned voff) { __builtin_amdgcn_raw_buffer_store_b128(v, rs, voff, 0, 16); }
DI unsigned addbf2(unsigned x, unsigned y) {
  const float lo = __uint_as_float(x << 16) + __uint_as_float(y << 16);
  const float hi = __uint_as_float(x & 0xffff0000u) + __uint_as_float(y & 0xffff0000u);
  return pk2(lo, hi);
}
DI unsigned umin2(unsigned a, unsigned b) { return a < b ? a : b; }
DI unsigned umax2(unsigned a, unsigned b) { return a > b ? a : b; }
DI unsigned umax4(u32x4 v) { return umax2(umax2(v[0], v[1]), umax2(v[2], v[3])); }
DI u32x4 clean(u32x4 v) { return u32x4{umin2(v[0], POISON - 1u), umin2(v[1], POISON - 1u), umin2(v[2], POISON - 1u), umin2(v[3], POISON - 1u)}; }
DI void at_write(gu32* p, unsigned v) { asm volatile("global_atomic_swap %0, %1, off" ::"v"(p), "v"(v) : "memory"); }
DI void lds_wait(LAS unsigned* p, unsigned want) {
#pragma unroll 1
  for (unsigned spins = 0; spins < (SPIN_CAP << 6); ++spins) {
    if ((unsigned)__builtin_amdgcn_readfirstlane(__hip_atomic_load(p, RLX_WG)) >= want) break;
    __builtin_amdgcn_s_sleep(1);
  }
  asm volatile("" ::: "memory");
}

__global__ void __launch_bounds__(768, 3) lin_kernel(const char* __restrict__ q, const char* __restrict__ k, const char* __restrict__ v,
                                                     const char* __restrict__ e, const char* __restrict__ f, char* ws,
                                                     char* __restrict__ out) {
  __shared__ __attribute__((aligned(1024))) char smem[L_BYTES];
  LAS char* L = (LAS char*)smem;
  LAS unsigned* flag = (LAS unsigned*)(L + L_FLAG);
  unsigned long long pc0;
  asm volatile("s_getpc_b64 %0" : "=s"(pc0));
  __builtin_amdgcn_sched_barrier(0);
  const int b = blockIdx.x, xcd = b & 7, jj = b >> 3, head = xcd * 4 + (jj >> 3), sub = jj & 7;
  const int tensor = sub >> 2, role = sub & 3;
  const int tid = threadIdx.x, lane = tid & 63, w = __builtin_amdgcn_readfirstlane(tid >> 6);
  const bool helper = role == 3;
  const int lw = w & 3;
  const unsigned ldsb = (unsigned)(size_t)L;
  if (w < 2) {
    const __amdgpu_buffer_rsrc_t rsC = __builtin_amdgcn_make_buffer_rsrc((char*)(pc0 & ~255ull), 0, CODE_BYTES, 0x00020000);
    asm volatile("s_nop 4");
    dma(ldsb + L_DUMP, lane * 16, rsC, (sub * 2 + w) * 1024);
  }
  __builtin_amdgcn_sched_barrier(0);
  const __amdgpu_buffer_rsrc_t rsK =
      __builtin_amdgcn_make_buffer_rsrc((char*)(tensor ? v : k) + (size_t)head * SKV * ROWB, 0, SKV * ROWB, 0x00020000);
  const __amdgpu_buffer_rsrc_t rsE = __builtin_amdgcn_make_buffer_rsrc((char*)(tensor ? f : e), 0, R * SKV * 2, 0x00020000);
  const unsigned rot = (jj >> 4) * 12u;
  unsigned sk = helper ? 1536u * ROWB : role * 128u + rot * 64u * ROWB, se = helper ? 3072u : rot * 128u;
  int pairs = helper ? 16 : 0;
  unsigned voK[2], voE[2];
#pragma unroll
  for (int x = 0; x < 2; ++x) {
    const int row = 8 * (2 * lw + x) + (lane >> 3);
    voK[x] = row * ROWB + (((lane & 7) ^ (((lane >> 4) & 1) << 2)) << 4);
    voE[x] = row * (SKV * 2) + (((lane & 7) ^ ((x << 2) | (lane >> 4))) << 4);
  }
  unsigned dsl = ldsb + lw * 2048;
  auto issue = [&]() {
    dma_nt(dsl, voK[0], rsK, sk);
    dma_nt(dsl + 1024, voK[1], rsK, sk);
    dma(dsl + 8192, voE[0], rsE, se);
    dma(dsl + 9216, voE[1], rsE, se);
    if (pairs > 0) {
      --pairs;
      sk += pairs & 1 ? 128u : pairs ? 64u * ROWB - 128u : 128u - 448u * ROWB;
      se += pairs & 1 ? 0u : pairs ? 128u : -896u;
    } else {
      sk += 64u * ROWB;
      se += 128u;
      if (se == 3072u) { se = 0u; sk -= 1536u * ROWB; }
    }
    dsl += SLOT;
    if (dsl >= ldsb + RING * SLOT) dsl -= RING * SLOT;
  };
  if ((w & 12) == 4) {
    if (sub == 1 || sub == 2) {
      const char* tb = sub == 1 ? (jj & 8 ? f : e) : ws + head * WS_HEAD + PAY_HEAD + 31 * 128;
      dma_nt(ldsb + L_DUMP, 0u, __builtin_amdgcn_make_buffer_rsrc((char*)tb, 0, 16, 0x00020000), 0);
    }
#pragma unroll
    for (int s = 0; s < RING - 1; ++s) issue();
  }
  __builtin_amdgcn_sched_barrier(0);
  const __amdgpu_buffer_rsrc_t rsP = __builtin_amdgcn_make_buffer_rsrc(ws + (size_t)head * WS_HEAD, 0, PAY_HEAD, 0x00020000);
  gu32* ctl = (gu32*)(ws + head * WS_HEAD + PAY_HEAD);

  const __amdgpu_buffer_rsrc_t rsQ =
      __builtin_amdgcn_make_buffer_rsrc((char*)q + ((size_t)head * SQ + sub * 256) * ROWB, 0, 256 * ROWB, 0x00020000);
  unsigned voQ[6];
  auto qprep = [&]() {
#pragma unroll
    for (int bb = 0; bb < 3; ++bb) {
      const int idx = 64 * bb + lane, row = idx / 24, c = idx - row * 24;
      voQ[bb] = row * 384 + (((c & 24) | ((c & 7) ^ (row >> 1))) << 4);
      voQ[3 + bb] = (voQ[bb] ^ 64u) + 3072u;
    }
    asm volatile("" : "+v"(voQ[0]), "+v"(voQ[1]), "+v"(voQ[2]), "+v"(voQ[3]), "+v"(voQ[4]), "+v"(voQ[5]));
  };
  auto qgroup = [&](int j) {
    const unsigned d = ldsb + (j < 8 ? L_SCR_LO : L_SCR_HI - 49152) + j * 6144;
#pragma unroll
    for (int i = 0; i < 6; ++i) dma_nt(d + i * 1024, voQ[i], rsQ, j * 6144);
  };

  if (__builtin_expect(w == 8, 0)) {
    const int l8 = lane & 7;
    gu32* nonce_p = ctl + l8;
    gu32* inbox_p = ctl + (1 + 2 * sub + (l8 >> 2)) * 32 + (l8 & 3);
    gu32* note_p = ctl + (1 + 2 * l8 + tensor) * 32 + role;
    const unsigned old = __hip_atomic_load(lane < 8 ? inbox_p : ctl + sub, RLX_AGENT);
    if (lane < 8) flag[lane] = 0u;
    const u32x4 pz = {POISON, POISON, POISON, POISON};
#pragma unroll 1
    for (int s = helper ? 0 : 2; s < 3; ++s) {
      const unsigned base = lane * 16 + tensor * 24576 + (helper ? PART_OFF + s * 4096 : role * 4096);
#pragma unroll
      for (int i = 0; i < 8; ++i) st_sc1(pz, rsP, base + (i >> 2) * 12288 + (i & 3) * 1024);
    }
    lgkm0();
    bars<1>();
    unsigned n = __builtin_amdgcn_readlane(old, 8) + 1u;
    while (__builtin_amdgcn_ballot_w64(old == n) != 0ull) ++n;
    if (lane == 0) at_write(ctl + sub, n);
    bars<9>();
    unsigned echoed = __hip_atomic_load(nonce_p, RLX_AGENT);
    bars<6>();
    vmcnt<0>();
    asm volatile("" : "+v"(echoed));
    bars<8>();
    if (lane < 8) at_write(note_p, echoed);
    auto refresh = [&]() {
      const unsigned cur = __hip_atomic_load(nonce_p, RLX_AGENT);
      if (lane < 8 && cur != echoed) at_write(note_p, cur);
      echoed = cur;
    };
    bars<1>();
    unsigned seen = 0u;
#pragma unroll 1
    for (unsigned spins = 0; spins < (SPIN_CAP >> 3); ++spins) {
      if (lane < 8) {
        unsigned c0, c1, c2;
#define POLL_STEP(NEW, OLD)                                                                                                        \
  "global_atomic_add " NEW ", %4, %5, off sc0\n\ts_sleep %6\n\ts_waitcnt vmcnt(2)\n\tv_cmp_eq_u32 vcc, %9, " OLD "\n\t"           \
  "s_or_b32 %3, %3, vcc_lo\n\tv_and_b32 " OLD ", %3, %10\n\tv_cmp_eq_u32 vcc, " OLD ", %10\n\t"                                  \
  "s_and_saveexec_b64 s[6:7], vcc\n\tds_write_b32 %7, %8\n\ts_mov_b64 exec, s[6:7]\n\ts_cmp_eq_u32 %3, 0xff\n\ts_cbranch_scc1 2f\n\t"
        asm volatile(
            "global_atomic_add %0, %4, %5, off sc0\n\ts_sleep %6\n\t"
            "global_atomic_add %1, %4, %5, off sc0\n\ts_sleep %6\n\t"
            "s_mov_b32 s4, 7\n"
            "1:\n\t" POLL_STEP("%2", "%0") POLL_STEP("%0", "%1") POLL_STEP("%1", "%2")
            "s_sub_u32 s4, s4, 1\n\ts_cbranch_scc0 1b\n"
            "2:\n\t"
            "s_waitcnt vmcnt(0) lgkmcnt(0)"
            : "=&v"(c0), "=&v"(c1), "=&v"(c2), "+s"(seen)
            : "v"(inbox_p), "v"(0u), "n"(POLLGAP), "v"(ldsb + L_FLAG + (F_GO + (l8 >> 2)) * 4), "v"(1u), "s"(n), "v"(l8 & 4 ? 0xf0u : 0x0fu)
            : "memory", "vcc", "scc", "s4", "s6", "s7");
      }
      seen = __builtin_amdgcn_readfirstlane(seen);
      refresh();
      if (seen == 0xffu) break;
    }
    if (lane < 2) __hip_atomic_store(flag + F_GO + lane, 1u, RLX_WG);
    return;
  }
  if (__builtin_expect(w == 9, 0)) {
    qprep();
    bars<NST + 1 - QSTART>();
    int j = 0;
#pragma unroll 1
    for (int i = 0; i < QSTART; ++i) {
      qgroup(j++);
      bars<1>();
    }
#pragma unroll 1
    for (; j < 8; ++j) {
      if ((unsigned)__builtin_amdgcn_readfirstlane(__hip_atomic_load(flag + F_ISS, RLX_WG)) < 8u) {
        vmcnt<W1 - 6>();
        if (j + 1 - W1 / 6 > 0 && lane == 0) __hip_atomic_store(flag + F_Q, (unsigned)(j + 1 - W1 / 6), RLX_WG);
      }
      qgroup(j);
    }
#define QTAIL(F, N)                                                        \
  vmcnt<6 * (N)>();                                                        \
  if (lane == 0) __hip_atomic_store(flag + (F), 8u - (N), RLX_WG);
    vmcnt<36>();
    if (lane == 0) __hip_atomic_fetch_max(flag + F_Q, 2u, RLX_WG);
    vmcnt<24>();
    if (lane == 0) __hip_atomic_fetch_max(flag + F_Q, 4u, RLX_WG);
    QTAIL(F_Q, 3) QTAIL(F_Q, 2) QTAIL(F_Q, 1) QTAIL(F_Q, 0)
    return;
  }
  if (__builtin_expect(w >= 10, 0)) {
    qprep();
    bars<NST + 1>();
    bool wide = false;
#pragma unroll 1
    for (unsigned spins = 0; spins < (SPIN_CAP << 6); ++spins) {
      wide = (unsigned)__builtin_amdgcn_readfirstlane(__hip_atomic_load(flag + F_ISS, RLX_WG)) >= 8u;
      if (wide || (unsigned)__builtin_amdgcn_readfirstlane(__hip_atomic_load(flag + F_Q, RLX_WG)) >= S2START) break;
      __builtin_amdgcn_s_sleep(1);
    }
#pragma unroll 1
    for (int x = 0; x < 4; ++x) {
      if (!wide) {
        vmcnt<W2 - 6>();
        wide = (unsigned)__builtin_amdgcn_readfirstlane(__hip_atomic_load(flag + F_ISS, RLX_WG)) >= 8u;
      }
      qgroup(8 + 2 * (w - 10) + (x & 1) + 4 * (x >> 1));
    }
    vmcnt<12>();
    if (lane == 0) __hip_atomic_store(flag + F_Q2 + (w - 10), 1u, RLX_WG);
    vmcnt<0>();
    if (lane == 0) __hip_atomic_store(flag + F_Q2 + (w - 10), 2u, RLX_WG);
    return;
  }

  const int q4 = (lane & 15) >> 2, p4 = lane & 3, r32 = lane & 31, hh = lane >> 5;
  const int ch = w >> 2;
  const unsigned go = lw * 1024 + lane * 16, gs = ch * 24576;
  unsigned dst[6];
#pragma unroll
  for (int i = 0; i < 6; ++i) {
    const unsigned d = 64 * (i % 3) + 32 * (i / 3) + (r32 & 19) + ((r32 & 4) << 1) + ((r32 & 8) >> 1), d6 = d / 6u;
    dst[i] = ch ? L_VP + (d - 6 * d6) * VT + lw * 1024 + hh * 512 + d6 * 16 : L_KP + i * 4096 + go;
  }
  asm volatile("" : "+v"(dst[0]), "+v"(dst[1]), "+v"(dst[2]), "+v"(dst[3]), "+v"(dst[4]), "+v"(dst[5]));
  static_assert(RING >= 4 && RING <= 6, "ring depth");
  if (ch) {
#pragma unroll 1
    for (int s = 0; s < NST - (RING - 1); ++s) {
      vmcnt<4 * (RING - 2)>();
      bars<1>();
      issue();
    }
    if (RING > 5) { vmcnt<16>(); bars<1>(); }
    if (RING > 4) { vmcnt<12>(); bars<1>(); }
    vmcnt<8>(); bars<1>();
    vmcnt<4>(); bars<1>();
    vmcnt<0>(); bars<1>();
  } else {
    const int rq = lw & 1, cq = lw >> 1;
    const unsigned kt = (8 * hh + q4) * 128 + ((cq ^ (q4 >> 1)) << 6) + ((lane >> 4) & 1) * 32 + (p4 & 1) * 16 + (p4 >> 1) * 8;
    unsigned et[4];
#pragma unroll
    for (int kk = 0; kk < 4; ++kk) et[kk] = 8192 + (32 * rq + r32) * 128 + (((2 * kk + hh) ^ ((r32 >> 1) & 7)) << 4);
    const unsigned fin_v = tensor ? 24576 + cq * 12288 + rq * 2048 + lane * 16 : rq * 12288 + cq * 2048 + lane * 16;
    f32x16 acc, aco;
#pragma unroll
    for (int i = 0; i < 16; ++i) { acc[i] = 0.f; aco[i] = 0.f; }
    auto pack = [&](const f32x16& x, int t) {
      return clean(u32x4{pk2(x[8 * t], x[8 * t + 1]), pk2(x[8 * t + 2], x[8 * t + 3]), pk2(x[8 * t + 4], x[8 * t + 5]), pk2(x[8 * t + 6], x[8 * t + 7])});
    };
    auto run = [&](auto ISV) {
      u32x4 ea[4], eb[4], oa[4], ob[4];
#pragma unroll
      for (int kk = 0; kk < 4; ++kk) { oa[kk] = u32x4{0u, 0u, 0u, 0u}; ob[kk] = u32x4{0u, 0u, 0u, 0u}; }
      int so = 0;
      auto mul = [&](f32x16& c, u32x4(&fa_)[4], u32x4(&fb_)[4]) {
#pragma unroll
        for (int kk = 0; kk < 4; ++kk) c = ISV.value ? mfma32(fb_[kk], fa_[kk], c) : mfma32(fa_[kk], fb_[kk], c);
      };
      auto stage = [&](u32x4(&na)[4], u32x4(&nb)[4], f32x16& c, u32x4(&fa_)[4], u32x4(&fb_)[4]) {
        bars<1>();
        LAS char* sl = L + so;
#pragma unroll
        for (int kk = 0; kk < 4; ++kk) {
          na[kk] = tr2(sl + kt + kk * 2048, sl + kt + kk * 2048 + 512);
          nb[kk] = *(LAS u32x4*)(sl + et[kk]);
        }
        __builtin_amdgcn_sched_barrier(0);
        mul(c, fa_, fb_);
        __builtin_amdgcn_sched_barrier(0);
        lgkm0();
        so += SLOT;
        if (so == RING * SLOT) so = 0;
      };
      u32x4 held[4];
#pragma unroll
      for (int i = 0; i < 4; ++i) held[i] = u32x4{0u, 0u, 0u, 0u};
#pragma unroll 1
      for (int s = 0; s < NST; s += 2) {
        stage(ea, eb, aco, oa, ob);
        if (helper && s == 16) {
          held[0] = pack(acc, 0); held[1] = pack(acc, 1); held[2] = pack(aco, 0); held[3] = pack(aco, 1);
#pragma unroll
          for (int i = 0; i < 16; ++i) { acc[i] = 0.f; aco[i] = 0.f; }
        }
        if (helper && s == LATEPUB) {
#pragma unroll
          for (int i = 0; i < 4; ++i) st_sc1(held[i], rsP, fin_v + PART_OFF + (i >> 1) * 4096 + (i & 1) * 1024);
        }
        stage(oa, ob, acc, ea, eb);
      }
      mul(aco, oa, ob);
    };
    if (tensor) run(BoolC<true>{});
    else run(BoolC<false>{});
    acc += aco;
    const unsigned fin_o = fin_v + (helper ? PART_OFF + 8192 : role * 4096);
    st_sc1(pack(acc, 0), rsP, fin_o);
    st_sc1(pack(acc, 1), rsP, fin_o + 1024);
    asm volatile("" ::: "memory");
  }
  bar();
  LAS char* scr = L + (w < 4 ? L_SCR_LO : L_SCR_HI) + lw * 12288;
  unsigned fa[4];
#pragma unroll
  for (int k4 = 0; k4 < 4; ++k4) fa[k4] = r32 * 384 + (((2 * k4 + hh) ^ ((r32 >> 1) & 7)) << 4);
  LAS unsigned* qflag = flag + (ch ? F_Q2 + (lw & 1) : F_Q);
  const unsigned qwant = ch ? (lw >> 1) + 1 : 2 * lw + 2;
  lds_wait(flag + F_GO + ch, 1u);
  if (lane == 0) __hip_atomic_fetch_add(flag + F_ISS, 1u, RLX_WG);
#pragma unroll 1
  for (unsigned spins = 0; spins < SPIN_CAP; ++spins) {
    asm volatile("" ::: "memory");
    u32x4 gv[12];
#pragma unroll
    for (int i = 0; i < 12; ++i) gv[i] = ld_sc1(rsP, go, gs + (i >> 1) * 4096 + (i & 1) * PART_OFF);
    __builtin_amdgcn_sched_barrier(0);
    unsigned mx = 0u;
#pragma unroll
    for (int i = 0; i < 6; ++i) {
      const u32x4 x = gv[2 * i], y = gv[2 * i + 1];
      mx = umax2(mx, umax2(umax4(x), umax4(y)));
      *(LAS u32x4*)(L + dst[i]) = u32x4{addbf2(x[0], y[0]), addbf2(x[1], y[1]), addbf2(x[2], y[2]), addbf2(x[3], y[3])};
      __builtin_amdgcn_sched_barrier(0);
    }
    if (__builtin_amdgcn_ballot_w64(mx == POISON) == 0ull) break;
    __builtin_amdgcn_s_sleep(RETRY_SLEEP);
  }
  lgkm0();
  if (lane == 0) __hip_atomic_fetch_add(flag + F_KP + ch, 1u, RLX_WG);
  lds_wait(qflag, qwant);
  u32x4 qf[12];
#pragma unroll
  for (int ks = 0; ks < 12; ++ks) qf[ks] = *(LAS u32x4*)(scr + fa[ks & 3] + (ks >> 2) * 128);
  lds_wait(flag + F_KP, 4u);

  const float cs = 0.07216878364870322f * 1.4426950408889634f;
  auto xmax = [](float x) {
    auto rr = __builtin_amdgcn_permlane32_swap(__float_as_uint(x), __float_as_uint(x), false, false);
    return fmaxf(__uint_as_float(rr[0]), __uint_as_float(rr[1]));
  };
  f32x16 s0, s1;
#pragma unroll
  for (int i = 0; i < 16; ++i) { s0[i] = 0.f; s1[i] = 0.f; }
  float m0, sum0 = 0.f;
  {
    LAS u32x4* kf = (LAS u32x4*)(L + L_KP) + lane;
    u32x4 ka[24];
#pragma unroll
    for (int i = 0; i < 6; ++i) ka[i] = kf[i * 64];
    __builtin_amdgcn_sched_barrier(0);
#pragma unroll
    for (int i = 0; i < 12; ++i) {
      ka[i + 6] = kf[(i + 6) * 64];
      s0 = mfma32(ka[i], qf[i], s0);
      __builtin_amdgcn_sched_barrier(0);
    }
#pragma unroll
    for (int i = 12; i < 24; ++i) {
      if (i + 6 < 24) ka[i + 6] = kf[(i + 6) * 64];
      s1 = mfma32(ka[i], qf[i - 12], s1);
      if (i == 13) {
        m0 = s0[0];
#pragma unroll
        for (int j = 1; j < 8; ++j) m0 = fmaxf(m0, s0[j]);
      }
      if (i == 14) {
#pragma unroll
        for (int j = 8; j < 16; ++j) m0 = fmaxf(m0, s0[j]);
      }
      if (i == 15) m0 = xmax(m0) * cs;
      if (i >= 16) {
#pragma unroll
        for (int j = 2 * (i - 16); j < 2 * (i - 16) + 2; ++j) { s0[j] = __builtin_amdgcn_exp2f(__builtin_fmaf(s0[j], cs, -m0)); sum0 += s0[j]; }
      }
      __builtin_amdgcn_sched_barrier(0);
    }
  }
  float m = s1[0];
#pragma unroll
  for (int i = 1; i < 16; ++i) m = fmaxf(m, s1[i]);
  const float mc = fmaxf(xmax(m) * cs, m0);
  const float a0 = __builtin_amdgcn_exp2f(m0 - mc);
  float sum = sum0 * a0;
#pragma unroll
  for (int i = 0; i < 16; ++i) { s1[i] = __builtin_amdgcn_exp2f(__builtin_fmaf(s1[i], cs, -mc)); sum += s1[i]; }
  {
    auto rr = __builtin_amdgcn_permlane32_swap(__float_as_uint(sum), __float_as_uint(sum), false, false);
    sum = __uint_as_float(rr[0]) + __uint_as_float(rr[1]);
  }
  const float inv = __builtin_amdgcn_rcpf(sum), inv0 = inv * a0;
  u32x4 pf[4];
#pragma unroll
  for (int t = 0; t < 2; ++t) {
    pf[t] = u32x4{pk2(s0[8 * t] * inv0, s0[8 * t + 1] * inv0), pk2(s0[8 * t + 2] * inv0, s0[8 * t + 3] * inv0),
                  pk2(s0[8 * t + 4] * inv0, s0[8 * t + 5] * inv0), pk2(s0[8 * t + 6] * inv0, s0[8 * t + 7] * inv0)};
    pf[2 + t] = u32x4{pk2(s1[8 * t] * inv, s1[8 * t + 1] * inv), pk2(s1[8 * t + 2] * inv, s1[8 * t + 3] * inv),
                      pk2(s1[8 * t + 4] * inv, s1[8 * t + 5] * inv), pk2(s1[8 * t + 6] * inv, s1[8 * t + 7] * inv)};
  }
  lds_wait(flag + F_VP, 4u);
  f32x16 o[6];
#pragma unroll
  for (int t = 0; t < 6; ++t)
#pragma unroll
    for (int i = 0; i < 16; ++i) o[t][i] = 0.f;
  {
    LAS char* vb = L + L_VP + lane * 16;
    u32x4 va[24];
#pragma unroll
    for (int i = 0; i < 6; ++i) va[i] = *(LAS u32x4*)(vb + (i % 6) * VT + (i / 6) * 1024);
    __builtin_amdgcn_sched_barrier(0);
#pragma unroll
    for (int i = 0; i < 24; ++i) {
      if (i + 6 < 24) va[i + 6] = *(LAS u32x4*)(vb + ((i + 6) % 6) * VT + ((i + 6) / 6) * 1024);
      o[i % 6] = mfma32(pf[i / 6], va[i], o[i % 6]);
      __builtin_amdgcn_sched_barrier(0);
    }
  }
  const __amdgpu_buffer_rsrc_t rsO =
      __builtin_amdgcn_make_buffer_rsrc(out + ((size_t)head * SQ + sub * 256 + w * 32) * ROWB, 0, 32 * ROWB, 0x00020000);
  const unsigned ov = hh * 4 * ROWB + r32 * 12;
#pragma unroll
  for (int j = 0; j < 16; ++j) {
    const u32x3 x = {pk2(o[0][j], o[1][j]), pk2(o[2][j], o[3][j]), pk2(o[4][j], o[5][j])};
    __builtin_amdgcn_raw_buffer_store_b96(x, rsO, ov + ((j & 3) + 8 * (j >> 2)) * ROWB, 0, 2);
  }
}

extern "C" void kernel_launch(void* const* d_in, const int* in_sizes, int n_in, void* d_out, int out_size, void* d_ws, size_t ws_size,
                              hipStream_t stream) {
  lin_kernel<<<256, 768, 0, stream>>>((const char*)d_in[0], (const char*)d_in[1], (const char*)d_in[2], (const char*)d_in[3],
                                      (const char*)d_in[4], (char*)d_ws, (char*)d_out);
}
```
